```python
import torch

def _softmax(x, axis=-1):
    return torch.softmax(x, dim=axis)

def _gelu(x):
    return torch.nn.functional.gelu(x, approximate='tanh')

def _silu(x):
    return torch.nn.functional.silu(x)

def _sigmoid(x):
    return torch.sigmoid(x)

def _layernorm(x, axis=-1, eps=1e-05):
    m = x.mean(dim=axis, keepdim=True)
    v = x.var(dim=axis, keepdim=True, unbiased=False)
    return (x - m) / torch.sqrt(v + eps)

def _scatter_add(tgt, idx, src, axis=0):
    return tgt.clone().index_add_(axis, idx, src)

def _segment_sum(src, seg, n):
    out = torch.zeros((n,) + src.shape[1:], dtype=src.dtype)
    return out.index_add_(0, seg, src)

def _pad(x, pw):
    flat = tuple((v for pair in reversed(pw) for v in pair))
    return torch.nn.functional.pad(x, flat)
kernel_name = 'interleave_layers__ssm_conv_h3__mixer_gmlp__prompt'

def setup_inputs():

    _w = lambda a: (lambda t: t.to(torch.bfloat16) if t.is_floating_point() else t)(torch.as_tensor(a))

    def _A_setup():
        B, S, D = (1, 2048, 640)
        ks = 4
        return {'q': _w((lambda *s: torch.randn(*s, dtype=torch.bfloat16) * 0.05)(B, S, D)), 'k': _w((lambda *s: torch.randn(*s, dtype=torch.bfloat16) * 0.8)(B, S, D)), 'v': _w((lambda *s: torch.randn(*s, dtype=torch.bfloat16) * 0.8)(B, S, D)), 'shift_k': _w((lambda *s: torch.randn(*s, dtype=torch.bfloat16) * 0.8)(D, ks) * 0.5), 'ssm_k': _w((lambda *s: torch.randn(*s, dtype=torch.bfloat16) * 0.8)(D, S) * (1.0 / S ** 0.5)), 'ssm_D': _w((lambda *s: torch.randn(*s, dtype=torch.bfloat16) * 0.8)(D))}
    _A_in = _A_setup()

    def _B_setup():
        B, S, D, F = (1, 2048, 640, 1536)
        Fh = F // 2
        return {'x': _w((lambda *s: torch.randn(*s, dtype=torch.bfloat16) * 0.05)(B, S, D)), 'w_in': _w((lambda *s: torch.randn(*s, dtype=torch.bfloat16) * 0.4)(D, 2 * Fh)), 'w_sp': _w(0.01 * (lambda *s: torch.randn(*s, dtype=torch.bfloat16) * 0.4)(S, S)), 'b_sp': _w(1.0 + 0.01 * (lambda *s: torch.randn(*s, dtype=torch.bfloat16) * 0.1)(S)), 'w_out': _w((lambda *s: torch.randn(*s, dtype=torch.bfloat16) * 0.1)(Fh, D))}
    _B_in = _B_setup()
    return {'A_q': _A_in['q'], 'A_k': _A_in['k'], 'A_v': _A_in['v'], 'A_shift_k': _A_in['shift_k'], 'A_ssm_k': _A_in['ssm_k'], 'A_ssm_D': _A_in['ssm_D'], 'B_x': _B_in['x'], 'B_w_in': _B_in['w_in'], 'B_w_sp': _B_in['w_sp'], 'B_b_sp': _B_in['b_sp'], 'B_w_out': _B_in['w_out']}

def reference(A_q, A_k, A_v, A_shift_k, A_ssm_k, A_ssm_D, B_x, B_w_in, B_w_sp, B_b_sp, B_w_out):

    def _A_ref(q, k, v, shift_k, ssm_k, ssm_D):
        B, S, D = q.shape
        ks = shift_k.shape[-1]
        k_pad = _pad(k, ((0, 0), (ks - 1, 0), (0, 0)))
        k_shift = torch.zeros_like(k)
        for t in range(ks):
            k_shift = k_shift + k_pad[:, t:t + S, :] * shift_k[:, ks - 1 - t][None, None, :]
        kv = k_shift * v
        L = 2 * S
        kv_pad = _pad(kv, ((0, 0), (0, S), (0, 0)))
        kf_pad = _pad(ssm_k, ((0, 0), (0, S)))
        kv_f = (lambda x, axes=(-2, -1): torch.fft.rfft2(x.float(), dim=axes))(kv_pad[:, None, :, :], axes=(1, 2))
        kf_f = (lambda x, axes=(-2, -1): torch.fft.rfft2(x.float(), dim=axes))(kf_pad[:, None, :], axes=(1, 2))
        kf_f = (lambda x, *d: x.permute(*d))(kf_f, 1, 2, 0)
        y_f = kv_f * kf_f[None, :, :, :]
        y = (lambda x, s, axes=(-2, -1): torch.fft.irfft2(x, s=s, dim=axes).to(torch.bfloat16))(y_f, (1, L), axes=(1, 2))[:, 0, :S, :]
        y = y + kv * ssm_D[None, None, :]
        return q * y

    def _B_ref(x, w_in, w_sp, b_sp, w_out):
        B, S, D = x.shape
        F = w_in.shape[-1]
        Fh = F // 2
        z = _gelu(torch.einsum('bsd,df->bsf', x, w_in))
        z1 = z[..., :Fh].clone()
        z2 = _layernorm(z[..., Fh:], axis=-1)
        g = torch.einsum('ts,bsf->btf', w_sp, z2) + b_sp[None, :, None]
        return torch.einsum('bsf,fd->bsd', z1 * g, w_out)
    x = A_q
    x = x + _A_ref(_layernorm(x, axis=-1), A_k, A_v, A_shift_k, A_ssm_k, A_ssm_D)
    x = x + _B_ref(_layernorm(x, axis=-1), B_w_in, B_w_sp, B_b_sp, B_w_out)
    x = x + _A_ref(_layernorm(x, axis=-1), A_k, A_v, A_shift_k, A_ssm_k, A_ssm_D)
    x = x + _B_ref(_layernorm(x, axis=-1), B_w_in, B_w_sp, B_b_sp, B_w_out)
    return x
```

```cpp
#include <hip/hip_runtime.h>
#include <hip/hip_bf16.h>
typedef __hip_bfloat16 bf16;
#define S 2048
#define D 640
#define F 1536
#define FH 768
__device__ __forceinline__ float rb(float x){ return __bfloat162float(__float2bfloat16(x)); }
__global__ void k_kv(const bf16* k, const bf16* v, const bf16* sk, bf16* kv){
  int i = blockIdx.x*256+threadIdx.x; if(i>=S*D) return;
  int s=i/D, d=i%D;
  float acc=0.f;
  for(int t=0;t<4;++t){ int ss=s+t-3; float kk = ss>=0? __bfloat162float(k[ss*D+d]):0.f;
    float p = rb(kk*__bfloat162float(sk[d*4+3-t])); acc = rb(acc+p);}
  kv[i]=__float2bfloat16(acc*__bfloat162float(v[i]));
}
__global__ void k_conv(const bf16* kv, const bf16* h, const bf16* Dv, bf16* y){
  int i = blockIdx.x*256+threadIdx.x; if(i>=S*D) return;
  int s=i/D, d=i%D;
  float acc=0.f;
  for(int j=0;j<=s;++j) acc += __bfloat162float(h[d*S+j])*__bfloat162float(kv[(s-j)*D+d]);
  float yy = rb(acc);
  float t = rb(__bfloat162float(kv[i])*__bfloat162float(Dv[d]));
  y[i]=__float2bfloat16(yy+t);
}
__device__ float wsum(float v){ for(int o=32;o;o>>=1) v+=__shfl_xor(v,o); return v; }
__global__ void k_lnA(const bf16* x, const bf16* y, bf16* x1, bf16* xn){
  int s=blockIdx.x, l=threadIdx.x;
  float v[10]; float sum=0;
  for(int i=0;i<10;++i){ v[i]=__bfloat162float(x[s*D+l+64*i]); sum+=v[i]; }
  float m=wsum(sum)/D; float q=0; for(int i=0;i<10;++i){ float t=v[i]-m; q+=t*t;} float var=wsum(q)/D;
  float mb=rb(m), sd=rb(sqrtf(rb(rb(var)+1e-5f)));
  sum=0;
  for(int i=0;i<10;++i){ float qn=rb(rb(v[i]-mb)/sd); float p=rb(qn*__bfloat162float(y[s*D+l+64*i])); v[i]=rb(v[i]+p); x1[s*D+l+64*i]=__float2bfloat16(v[i]); sum+=v[i]; }
  m=wsum(sum)/D; q=0; for(int i=0;i<10;++i){ float t=v[i]-m; q+=t*t;} var=wsum(q)/D;
  mb=rb(m); sd=rb(sqrtf(rb(rb(var)+1e-5f)));
  for(int i=0;i<10;++i){ xn[s*D+l+64*i]=__float2bfloat16(rb(v[i]-mb)/sd); }
}
__global__ void k_gemm1(const bf16* xn, const bf16* w, bf16* z){
  int i = blockIdx.x*256+threadIdx.x; if(i>=S*F) return;
  int s=i/F, f=i%F; float acc=0;
  for(int d=0;d<D;++d) acc+=__bfloat162float(xn[s*D+d])*__bfloat162float(w[d*F+f]);
  float a=rb(acc);
  float u=0.7978845608028654f*(a+0.044715f*a*a*a);
  z[i]=__float2bfloat16(0.5f*a*(1.f+tanhf(u)));
}
__global__ void k_lnz(const bf16* z, bf16* z2){
  int s=blockIdx.x, l=threadIdx.x;
  float v[12]; float sum=0;
  for(int i=0;i<12;++i){ v[i]=__bfloat162float(z[s*F+FH+l+64*i]); sum+=v[i]; }
  float m=wsum(sum)/FH; float q=0; for(int i=0;i<12;++i){ float t=v[i]-m; q+=t*t;} float var=wsum(q)/FH;
  float mb=rb(m), sd=rb(sqrtf(rb(rb(var)+1e-5f)));
  for(int i=0;i<12;++i) z2[s*FH+l+64*i]=__float2bfloat16(rb(v[i]-mb)/sd);
}
__global__ void k_gemm2(const bf16* wsp, const bf16* z2, const bf16* b, const bf16* z, bf16* h){
  int i = blockIdx.x*256+threadIdx.x; if(i>=S*FH) return;
  int t=i/FH, f=i%FH; float acc=0;
  for(int s=0;s<S;++s) acc+=__bfloat162float(wsp[t*S+s])*__bfloat162float(z2[s*FH+f]);
  float g=rb(rb(acc)+__bfloat162float(b[t]));
  h[i]=__float2bfloat16(g*__bfloat162float(z[t*F+f]));
}
__global__ void k_gemm3(const bf16* h, const bf16* w, const bf16* x1, bf16* out){
  int i = blockIdx.x*256+threadIdx.x; if(i>=S*D) return;
  int s=i/D, d=i%D; float acc=0;
  for(int f=0;f<FH;++f) acc+=__bfloat162float(h[s*FH+f])*__bfloat162float(w[f*D+d]);
  out[i]=__float2bfloat16(rb(acc)+__bfloat162float(x1[i]));
}
extern "C" void kernel_launch(void* const* d_in, const int* in_sizes, int n_in,
                              void* d_out, int out_size, void* d_ws, size_t ws_size,
                              hipStream_t stream) {
  const bf16* q=(const bf16*)d_in[0]; const bf16* k=(const bf16*)d_in[1]; const bf16* v=(const bf16*)d_in[2];
  const bf16* sk=(const bf16*)d_in[3]; const bf16* h=(const bf16*)d_in[4]; const bf16* Dv=(const bf16*)d_in[5];
  const bf16* win=(const bf16*)d_in[7]; const bf16* wsp=(const bf16*)d_in[8]; const bf16* bsp=(const bf16*)d_in[9]; const bf16* wout=(const bf16*)d_in[10];
  bf16* ws=(bf16*)d_ws;
  bf16* kv=ws; bf16* y=kv+S*D; bf16* x1=y+S*D; bf16* xn=x1+S*D; bf16* x2=xn+S*D; bf16* z=x2+S*D; bf16* z2=z+S*F; bf16* hh=z2+S*FH;
  k_kv<<<S*D/256,256,0,stream>>>(k,v,sk,kv);
  k_conv<<<S*D/256,256,0,stream>>>(kv,h,Dv,y);
  const bf16* x=q;
  for(int l=0;l<2;++l){
    k_lnA<<<S,64,0,stream>>>(x,y,x1,xn);
    k_gemm1<<<S*F/256,256,0,stream>>>(xn,win,z);
    k_lnz<<<S,64,0,stream>>>(z,z2);
    k_gemm2<<<S*FH/256,256,0,stream>>>(wsp,z2,bsp,z,hh);
    bf16* o = l==0? x2 : (bf16*)d_out;
    k_gemm3<<<S*D/256,256,0,stream>>>(hh,wout,x1,o);
    x=x2;
  }
}
```

```cpp
#include <hip/hip_runtime.h>
#include <hip/hip_bf16.h>
typedef unsigned short u16;
typedef unsigned int u32;
typedef unsigned long long u64;
typedef short bf16x8 __attribute__((ext_vector_type(8)));
typedef short s16x4 __attribute__((ext_vector_type(4)));
typedef float f32x4 __attribute__((ext_vector_type(4)));
typedef float f32x2 __attribute__((ext_vector_type(2)));
typedef unsigned u32x4 __attribute__((ext_vector_type(4)));
typedef unsigned u32x2 __attribute__((ext_vector_type(2)));
typedef __bf16 bf16x2_t __attribute__((ext_vector_type(2)));
#define LAS __attribute__((address_space(3)))
#define GAS __attribute__((address_space(1)))
#define DEV __device__ __forceinline__

constexpr int S = 2048, D = 640, F = 1536, FH = 768;
constexpr size_t SZX = (size_t)S * D * 2;
constexpr size_t OFF_UR = 0;
constexpr size_t OFF_X0F = OFF_UR + SZX;
constexpr size_t OFF_YF = OFF_X0F + SZX;
constexpr size_t OFF_X1F = OFF_YF + SZX;
constexpr size_t OFF_X2F = OFF_X1F + SZX;
constexpr size_t OFF_WINF = OFF_X2F + SZX;
constexpr size_t OFF_WSPF = OFF_WINF + (size_t)D * F * 2;
constexpr size_t OFF_WOUTF = OFF_WSPF + (size_t)S * S * 2;
constexpr size_t OFF_CS = OFF_WOUTF + (size_t)FH * D * 2;
constexpr size_t OFF_CSP = OFF_CS + 6144;
constexpr size_t OFF_XST = OFF_CSP + 20 * 6144;
constexpr size_t OFF_Z1F = OFF_XST + (size_t)S * 64;
constexpr size_t OFF_Z2F = OFF_Z1F + (size_t)S * FH * 2;
constexpr size_t OFF_HF = OFF_Z2F + (size_t)S * FH * 2;
constexpr size_t OFF_ZG = OFF_HF + (size_t)S * FH * 2;

DEV u32 pack2(float a, float b) { f32x2 v = {a, b}; bf16x2_t r = __builtin_convertvector(v, bf16x2_t); return __builtin_bit_cast(u32, r); }
DEV float blo(u32 x) { return __builtin_bit_cast(float, x << 16); }
DEV float bhi(u32 x) { return __builtin_bit_cast(float, x & 0xffff0000u); }
DEV float b2f(u16 x) { return __builtin_bit_cast(float, (u32)x << 16); }
DEV float rbf(float x) { return blo(pack2(x, 0.f)); }
DEV f32x4 mfma(u32x4 a, u32x4 b, f32x4 c) {
  return __builtin_amdgcn_mfma_f32_16x16x32_bf16(__builtin_bit_cast(bf16x8, a), __builtin_bit_cast(bf16x8, b), c, 0, 0, 0);
}
DEV u32x2 tr_read(const void* p) {
  s16x4 v = __builtin_amdgcn_ds_read_tr16_b64_v4i16((s16x4 LAS*)p);
  return __builtin_bit_cast(u32x2, v);
}
template <int CTRL> DEV float dpp_add(float x) {
  return x + __builtin_bit_cast(float, __builtin_amdgcn_update_dpp(0, __builtin_bit_cast(int, x), CTRL, 0xf, 0xf, false));
}
DEV float row16_sum(float x) {
  x = dpp_add<0x128>(x); x = dpp_add<0x124>(x); x = dpp_add<0x122>(x); x = dpp_add<0x121>(x); return x;
}

__global__ __launch_bounds__(512) void k_prep(const u16* __restrict__ q, const u16* __restrict__ k, const u16* __restrict__ v,
                                              const u16* __restrict__ sk, const u16* __restrict__ win, char* __restrict__ ws) {
  __shared__ __attribute__((aligned(16))) u16 sm[80 * 72 + 2 * 32 * 72];
  u16* lt = sm;
  const int b = blockIdx.x, tid = threadIdx.x, wave = tid >> 6, lane = tid & 63;
  const int rb = b >> 3, cg = b & 7;
  if (tid < 320) {
    const int cq = tid % 20, rq = tid / 20;
    const int d0 = cg * 80 + cq * 4, s0 = rb * 64 + rq * 4;
    u32x2 kk[7], vv[4];
#pragma unroll
    for (int i = 0; i < 7; ++i) {
      int s = s0 - 3 + i;
      u32x2 z = {0, 0};
      kk[i] = (s >= 0) ? *(const u32x2*)(k + (size_t)s * D + d0) : z;
    }
#pragma unroll
    for (int i = 0; i < 4; ++i) vv[i] = *(const u32x2*)(v + (size_t)(s0 + i) * D + d0);
    u32x4 w0 = *(const u32x4*)(sk + d0 * 4), w1 = *(const u32x4*)(sk + d0 * 4 + 8);
#pragma unroll
    for (int ch = 0; ch < 4; ++ch) {
      u32 wa = ch < 2 ? w0[(ch & 1) * 2] : w1[(ch & 1) * 2], wb = ch < 2 ? w0[(ch & 1) * 2 + 1] : w1[(ch & 1) * 2 + 1];
      float c0 = blo(wa), c1 = bhi(wa), c2 = blo(wb), c3 = bhi(wb);
      float kf[7];
#pragma unroll
      for (int i = 0; i < 7; ++i) kf[i] = (ch & 1) ? bhi(kk[i][ch >> 1]) : blo(kk[i][ch >> 1]);
      float o[4];
#pragma unroll
      for (int r = 0; r < 4; ++r) {
        float ks = kf[r] * c3;
        ks = fmaf(kf[r + 1], c2, ks); ks = fmaf(kf[r + 2], c1, ks); ks = fmaf(kf[r + 3], c0, ks);
        float vf = (ch & 1) ? bhi(vv[r][ch >> 1]) : blo(vv[r][ch >> 1]);
        o[r] = rbf(ks) * vf;
      }
      u32x2 pk = {pack2(o[3], o[2]), pack2(o[1], o[0])};
      *(u32x2*)(lt + (cq * 4 + ch) * 72 + 60 - 4 * rq) = pk;
    }
  } else if (wave == 5) {
    const int row = b * 8 + (lane & 7), cc = lane >> 3;
    u32x4 x[10];
#pragma unroll
    for (int i = 0; i < 10; ++i) x[i] = *(const u32x4*)(q + (size_t)row * D + (cc + 8 * i) * 8);
    float s1 = 0.f, s2 = 0.f;
#pragma unroll
    for (int i = 0; i < 10; ++i) {
#pragma unroll
      for (int e = 0; e < 4; ++e) { float a = blo(x[i][e]), c = bhi(x[i][e]); s1 += a + c; s2 = fmaf(a, a, fmaf(c, c, s2)); }
      int c = cc + 8 * i, ks = c >> 2, g = c & 3;
      *(u32x4*)(ws + OFF_X0F + ((size_t)(((row >> 4) * 20 + ks) * 64 + g * 16 + (row & 15))) * 16) = x[i];
    }
    s1 += __shfl_xor(s1, 8); s2 += __shfl_xor(s2, 8);
    s1 += __shfl_xor(s1, 16); s2 += __shfl_xor(s2, 16);
    s1 += __shfl_xor(s1, 32); s2 += __shfl_xor(s2, 32);
    f32x2 st = {cc == 0 ? s1 : 0.f, cc == 0 ? s2 : 0.f};
    *(f32x2*)(ws + OFF_XST + (size_t)row * 64 + cc * 8) = st;
  } else {
    u32x4 ones = {0xffffffffu, 0xffffffffu, 0xffffffffu, 0xffffffffu};
    *(u32x4*)(ws + OFF_ZG + (size_t)b * 2048 + (tid - 384) * 16) = ones;
    if (b < 240) {
      const int ks = b / 12, n0 = (b % 12) * 128 + (wave - 6) * 64;
      u16* lw = sm + 80 * 72 + (wave - 6) * 32 * 72;
      u32x4 t[4];
#pragma unroll
      for (int i = 0; i < 4; ++i) t[i] = *(const u32x4*)(win + (size_t)(ks * 32 + i * 8 + (lane >> 3)) * F + n0 + (lane & 7) * 8);
#pragma unroll
      for (int i = 0; i < 4; ++i) *(u32x4*)(lw + (i * 8 + (lane >> 3)) * 72 + (lane & 7) * 8) = t[i];
      __builtin_amdgcn_fence(__ATOMIC_RELEASE, "wavefront");
      __builtin_amdgcn_wave_barrier();
      __builtin_amdgcn_fence(__ATOMIC_ACQUIRE, "wavefront");
      const int g = lane >> 4, qq = (lane & 15) >> 2, p = lane & 3;
#pragma unroll
      for (int nt = 0; nt < 4; ++nt) {
        u32x2 a = tr_read(lw + (8 * g + qq) * 72 + nt * 16 + 4 * p);
        u32x2 c = tr_read(lw + (8 * g + 4 + qq) * 72 + nt * 16 + 4 * p);
        u32x4 o = {a[0], a[1], c[0], c[1]};
        int ntg = (n0 >> 4) + nt;
        *(u32x4*)(ws + OFF_WINF + ((size_t)(ntg * 20 + ks) * 64 + lane) * 16) = o;
        float cs = blo(a[0]) + bhi(a[0]) + blo(a[1]) + bhi(a[1]) + blo(c[0]) + bhi(c[0]) + blo(c[1]) + bhi(c[1]);
        cs += __shfl_xor(cs, 16); cs += __shfl_xor(cs, 32);
        if (lane < 16) *(float*)(ws + OFF_CSP + ((size_t)ks * F + ntg * 16 + lane) * 4) = cs;
      }
    }
  }
  __syncthreads();
  for (int i = tid; i < 640; i += 512) {
    int ch = i >> 3, c = i & 7;
    u32x4 t = *(const u32x4*)(lt + ch * 72 + c * 8);
    *(u32x4*)(ws + OFF_UR + ((size_t)(cg * 80 + ch) * S + (1984 - 64 * rb) + c * 8) * 2) = t;
  }
}

constexpr int CV_UR = 4608, CV_H = 4160, CV_WAVE = CV_UR + 4 * CV_H;
constexpr int CV_Y = 4 * CV_WAVE, CV_YS = 4096 + 64;
constexpr int CV_LDS = CV_Y + 4 * CV_YS;

__global__ __launch_bounds__(256, 1) void k_conv(const u16* __restrict__ hk, const u16* __restrict__ Dv, const u16* __restrict__ wsp,
                                                 const u16* __restrict__ wout, char* __restrict__ ws) {
  extern __shared__ __attribute__((aligned(16))) char smem[];
  const int b = blockIdx.x, tid = threadIdx.x, w = tid >> 6, lane = tid & 63;
  if (b < 160) {
    const int d = b * 4 + w;
    char* lur = smem + w * CV_WAVE;
    char* lh = lur + CV_UR;
    {
      const u16* ur = (const u16*)(ws + OFF_UR) + (size_t)d * S;
      const u16* hp = hk + (size_t)d * S;
      u32x4 uu[4], hh[4]; u32x2 hn[4];
#pragma unroll
      for (int i = 0; i < 4; ++i) {
        int ci = lane + 64 * i;
        hh[i] = *(const u32x4*)(hp + ci * 8);
        u32x2 z = {0, 0};
        hn[i] = (ci < 255) ? *(const u32x2*)(hp + ci * 8 + 8) : z;
        uu[i] = *(const u32x4*)(ur + ci * 8);
      }
      u32x4 z4 = {0, 0, 0, 0};
      if (lane < 32) *(u32x4*)(lur + 4096 + lane * 16) = z4;
      {
        u32 f0 = __shfl(hh[0][0], 0), f1 = __shfl(hh[0][1], 0);
        if (lane < 8) {
          int qc = lane >> 1, which = lane & 1;
          u32x4 o = z4;
          if (which) {
            if (qc == 1) o[3] = f0 << 16;
            else if (qc == 2) o[3] = f0;
            else if (qc == 3) { o[2] = f0 << 16; o[3] = __builtin_amdgcn_alignbit(f1, f0, 16); }
          }
          *(u32x4*)(lh + qc * CV_H + which * 16) = o;
        }
      }
#pragma unroll
      for (int i = 0; i < 4; ++i) {
        int ci = lane + 64 * i;
        *(u32x4*)(lur + ci * 16) = uu[i];
        u32 w0 = hh[i][0], w1 = hh[i][1], w2 = hh[i][2], w3 = hh[i][3], w4 = hn[i][0], w5 = hn[i][1];
        u32 a01 = __builtin_amdgcn_alignbit(w1, w0, 16), a12 = __builtin_amdgcn_alignbit(w2, w1, 16), a23 = __builtin_amdgcn_alignbit(w3, w2, 16),
            a34 = __builtin_amdgcn_alignbit(w4, w3, 16), a45 = __builtin_amdgcn_alignbit(w5, w4, 16);
        u32x4 c0 = {w0, w1, w2, w3}, c1 = {a01, a12, a23, a34}, c2 = {w1, w2, w3, w4}, c3 = {a12, a23, a34, a45};
        *(u32x4*)(lh + 0 * CV_H + 32 + ci * 16) = c0;
        *(u32x4*)(lh + 1 * CV_H + 32 + ci * 16) = c1;
        *(u32x4*)(lh + 2 * CV_H + 32 + ci * 16) = c2;
        *(u32x4*)(lh + 3 * CV_H + 32 + ci * 16) = c3;
      }
    }
    __syncthreads();
    const int n = lane & 15, g = lane >> 4;
    const int qc = (1 + n) & 3;
    const char* abase = lh + qc * CV_H + (1 + n + 8 * g - qc) * 2;
    u32x4 A[64];
#pragma unroll
    for (int cc = 0; cc < 64; ++cc) {
      u32x2 lo = *(const u32x2*)(abase + cc * 64), hi = *(const u32x2*)(abase + cc * 64 + 8);
      A[cc] = (u32x4){lo[0], lo[1], hi[0], hi[1]};
    }
    f32x4 acc[8];
#pragma unroll
    for (int t = 0; t < 8; ++t) acc[t] = (f32x4){0, 0, 0, 0};
    const char* bbase = lur + (2032 - 16 * n + 8 * g) * 2;
#pragma unroll
    for (int dl = 0; dl < 8; ++dl) {
      u32x4 B[8];
#pragma unroll
      for (int i = 0; i < 8; ++i) B[i] = *(const u32x4*)(bbase - (8 * dl - i) * 64);
#pragma unroll
      for (int C = 0; C + dl < 8; ++C)
#pragma unroll
        for (int i = 0; i < 8; ++i) acc[C + dl] = mfma(A[8 * C + i], B[i], acc[C + dl]);
    }
    const float Dd = b2f(Dv[d]);
    char* ly = smem + CV_Y;
#pragma unroll
    for (int t = 0; t < 8; ++t) {
      int sb = 256 * t + 16 * n + 4 * g;
      u32x2 u = *(const u32x2*)(lur + (2044 - sb) * 2);
      float u3 = blo(u[0]), u2 = bhi(u[0]), u1 = blo(u[1]), u0 = bhi(u[1]);
      float y0 = rbf(acc[t][0]) + rbf(u0 * Dd), y1 = rbf(acc[t][1]) + rbf(u1 * Dd), y2 = rbf(acc[t][2]) + rbf(u2 * Dd), y3 = rbf(acc[t][3]) + rbf(u3 * Dd);
      u32x2 o = {pack2(y0, y1), pack2(y2, y3)};
      *(u32x2*)(ly + w * CV_YS + sb * 2) = o;
    }
    __syncthreads();
    {
      const int d0 = b * 4, ks = d0 >> 5, gg = (d0 & 31) >> 3, j = d0 & 7;
      const int qq = (lane & 15) >> 2, p = lane & 3;
#pragma unroll
      for (int i = 0; i < 8; ++i) {
        int sbk = (w * 8 + i) * 4 + g;
        u32x2 o = tr_read(ly + qq * CV_YS + (16 * sbk + 4 * p) * 2);
        *(u32x2*)(ws + OFF_YF + (((size_t)(sbk * 20 + ks) * 64 + gg * 16 + n) * 8 + j) * 2) = o;
      }
    }
  } else {
    const int wg = (b - 160) * 4 + w;
    for (int i0 = 0; i0 < 24; i0 += 8) {
      u32x4 t[8];
#pragma unroll
      for (int i = 0; i < 8; ++i) {
        int u = wg + 384 * (i0 + i);
        if (u < 8192) t[i] = *(const u32x4*)(wsp + (size_t)((u >> 5) * 8 + (lane >> 3)) * S + (u & 31) * 64 + (lane & 7) * 8);
      }
#pragma unroll
      for (int i = 0; i < 8; ++i) {
        int u = wg + 384 * (i0 + i);
        if (u < 8192) {
          int row = (u >> 5) * 8 + (lane >> 3), c8 = lane & 7, ks = (u & 31) * 2 + (c8 >> 2), g = c8 & 3;
          *(u32x4*)(ws + OFF_WSPF + ((size_t)((((row >> 6) * 64 + ks) * 4 + ((row >> 4) & 3)) * 64 + g * 16 + (row & 15))) * 16) = t[i];
        }
      }
    }
    if (wg < 240) {
      const int ks = wg / 10, n0 = (wg % 10) * 64;
      u16* lw = (u16*)(smem + w * 8192);
      u32x4 t[4];
#pragma unroll
      for (int i = 0; i < 4; ++i) t[i] = *(const u32x4*)(wout + (size_t)(ks * 32 + i * 8 + (lane >> 3)) * D + n0 + (lane & 7) * 8);
#pragma unroll
      for (int i = 0; i < 4; ++i) *(u32x4*)(lw + (i * 8 + (lane >> 3)) * 72 + (lane & 7) * 8) = t[i];
      __builtin_amdgcn_fence(__ATOMIC_RELEASE, "wavefront");
      __builtin_amdgcn_wave_barrier();
      __builtin_amdgcn_fence(__ATOMIC_ACQUIRE, "wavefront");
      const int g = lane >> 4, qq = (lane & 15) >> 2, p = lane & 3;
#pragma unroll
      for (int nt = 0; nt < 4; ++nt) {
        u32x2 a = tr_read(lw + (8 * g + qq) * 72 + nt * 16 + 4 * p);
        u32x2 c = tr_read(lw + (8 * g + 4 + qq) * 72 + nt * 16 + 4 * p);
        u32x4 o = {a[0], a[1], c[0], c[1]};
        int ntg = (n0 >> 4) + nt;
        *(u32x4*)(ws + OFF_WOUTF + ((size_t)(((ntg / 5) * 24 + ks) * 5 + ntg % 5) * 64 + lane) * 16) = o;
      }
    } else if (wg < 264) {
      int col = (wg - 240) * 64 + lane;
      const float* csp = (const float*)(ws + OFF_CSP);
      float t[20], s = 0.f;
#pragma unroll
      for (int i = 0; i < 20; ++i) t[i] = csp[i * F + col];
#pragma unroll
      for (int i = 0; i < 20; ++i) s += t[i];
      *(float*)(ws + OFF_CS + col * 4) = s;
    }
  }
}

DEV float gelu_t(float a) {
  const float k0 = -2.f * 0.7978845608028654f * 1.4426950408889634f;
  float t = a * a;
  float e = __builtin_amdgcn_exp2f(a * fmaf(t, k0 * 0.044715f, k0));
  return a * __builtin_amdgcn_rcpf(1.f + e);
}
constexpr int G1_ST = 81920;
constexpr int G1_LDS = G1_ST + 1024 + 1024;

__global__ __launch_bounds__(512, 2) void k_gemm1(const char* __restrict__ xin, char* __restrict__ xout, char* __restrict__ ws, int layer) {
  extern __shared__ __attribute__((aligned(16))) char smem[];
  const int b = blockIdx.x, tid = threadIdx.x, wv = tid >> 6, lane = tid & 63;
  const int xcd = b & 7, idx = b >> 3;
  const int p = xcd * 4 + (idx >> 3), c = idx & 7;
  const int w = wv & 3, kh = wv >> 2;
  const int r16 = lane & 15, g = lane >> 4;
  const size_t aoff = ((size_t)((p * 4 + w) * 20 + kh * 10) * 64 + lane) * 16;
  u32x4 xv[10], yv[10];
#pragma unroll
  for (int i = 0; i < 10; ++i) xv[i] = *(const u32x4*)(xin + aoff + i * 1024);
#pragma unroll
  for (int i = 0; i < 10; ++i) yv[i] = *(const u32x4*)(ws + OFF_YF + aoff + i * 1024);
  const int row = p * 64 + w * 16 + r16;
  f32x4 stp = *(const f32x4*)(ws + OFF_XST + (size_t)row * 64 + g * 16);
  const int nt0 = (w < 2 ? 0 : 48) + 6 * c + 3 * (w & 1);
  u32x4 B[3][10];
#pragma unroll
  for (int i = 0; i < 10; ++i)
#pragma unroll
    for (int j = 0; j < 3; ++j) B[j][i] = *(const u32x4*)(ws + OFF_WINF + ((size_t)((nt0 + j) * 20 + kh * 10 + i) * 64 + lane) * 16);
  float s1 = stp[0] + stp[2], s2 = stp[1] + stp[3];
  s1 += __shfl_xor(s1, 16); s2 += __shfl_xor(s2, 16);
  s1 += __shfl_xor(s1, 32); s2 += __shfl_xor(s2, 32);
  const float mean = s1 * (1.f / D);
  const float rs = __builtin_amdgcn_rsqf(fmaxf(s2 * (1.f / D) - mean * mean, 0.f) + 1e-5f);
  const float mr = -mean * rs;
  float t1 = 0.f, t2 = 0.f;
#pragma unroll
  for (int i = 0; i < 10; ++i) {
    u32x4 o;
#pragma unroll
    for (int e = 0; e < 4; ++e) {
      float xa = blo(xv[i][e]), xb = bhi(xv[i][e]), ya = blo(yv[i][e]), yb = bhi(yv[i][e]);
      float oa = fmaf(fmaf(xa, rs, mr), ya, xa), ob = fmaf(fmaf(xb, rs, mr), yb, xb);
      u32 pk = pack2(oa, ob);
      o[e] = pk;
      float ra = blo(pk), rb2 = bhi(pk);
      t1 += ra + rb2; t2 = fmaf(ra, ra, fmaf(rb2, rb2, t2));
    }
    *(u32x4*)(smem + ((kh * 10 + i) * 4 + w) * 1024 + lane * 16) = o;
    if ((i & 7) == c || (i >= 8 && false)) {}
    if (((kh * 10 + i) & 7) == c) *(u32x4*)(xout + aoff + i * 1024) = o;
  }
  t1 += __shfl_xor(t1, 16); t2 += __shfl_xor(t2, 16);
  t1 += __shfl_xor(t1, 32); t2 += __shfl_xor(t2, 32);
  if (lane < 16) *(f32x2*)(smem + G1_ST + (kh * 64 + w * 16 + lane) * 8) = (f32x2){t1, t2};
  __syncthreads();
  f32x4 acc[4][3];
#pragma unroll
  for (int m = 0; m < 4; ++m)
#pragma unroll
    for (int j = 0; j < 3; ++j) acc[m][j] = (f32x4){0, 0, 0, 0};
  if (w < 2) {
#pragma unroll
    for (int i = 0; i < 10; ++i) {
      u32x4 A[4];
#pragma unroll
      for (int m = 0; m < 4; ++m) A[m] = *(const u32x4*)(smem + ((kh * 10 + i) * 4 + m) * 1024 + lane * 16);
#pragma unroll
      for (int m = 0; m < 4; ++m)
#pragma unroll
        for (int j = 0; j < 3; ++j) acc[m][j] = mfma(B[j][i], A[m], acc[m][j]);
    }
  } else {
#pragma unroll
    for (int i = 0; i < 10; ++i) {
      u32x4 A[4];
#pragma unroll
      for (int m = 0; m < 4; ++m) A[m] = *(const u32x4*)(smem + ((kh * 10 + i) * 4 + m) * 1024 + lane * 16);
#pragma unroll
      for (int m = 0; m < 4; ++m)
#pragma unroll
        for (int j = 0; j < 3; ++j) acc[m][j] = mfma(A[m], B[j][i], acc[m][j]);
    }
  }
  __syncthreads();
#pragma unroll
  for (int m = 0; m < 2; ++m)
#pragma unroll
    for (int j = 0; j < 3; ++j) {
      f32x4 give = kh ? acc[m][j] : acc[2 + m][j];
      *(f32x4*)(smem + ((wv * 6 + m * 3 + j) * 64 + lane) * 16) = give;
    }
  __syncthreads();
  f32x4 fin[2][3];
#pragma unroll
  for (int m = 0; m < 2; ++m)
#pragma unroll
    for (int j = 0; j < 3; ++j) {
      f32x4 got = *(const f32x4*)(smem + (((wv ^ 4) * 6 + m * 3 + j) * 64 + lane) * 16);
      f32x4 keep = kh ? acc[2 + m][j] : acc[m][j];
      fin[m][j] = keep + got;
    }
  const float* cs = (const float*)(ws + OFF_CS);
  if (w < 2) {
#pragma unroll
    for (int m = 0; m < 2; ++m) {
      int rl = (2 * kh + m) * 16 + r16;
      f32x2 a = *(const f32x2*)(smem + G1_ST + rl * 8), a2 = *(const f32x2*)(smem + G1_ST + (64 + rl) * 8);
      float mn = (a[0] + a2[0]) * (1.f / D);
      float r1 = __builtin_amdgcn_rsqf(fmaxf((a[1] + a2[1]) * (1.f / D) - mn * mn, 0.f) + 1e-5f);
#pragma unroll
      for (int j = 0; j < 3; ++j) {
        f32x4 cv = *(const f32x4*)(cs + (nt0 + j) * 16 + 4 * g);
        float z[4];
#pragma unroll
        for (int r = 0; r < 4; ++r) z[r] = gelu_t(fmaf(-mn, cv[r], fin[m][j][r]) * r1);
        int f = (nt0 + j) * 16 + 4 * g;
        size_t off = ((size_t)(((p * 24 + (f >> 5)) * 4 + 2 * kh + m) * 64 + ((f & 31) >> 3) * 16 + r16) * 8 + (f & 7)) * 2;
        *(u32x2*)(ws + OFF_Z1F + off) = (u32x2){pack2(z[0], z[1]), pack2(z[2], z[3])};
      }
    }
  } else {
    float cv[3];
#pragma unroll
    for (int j = 0; j < 3; ++j) cv[j] = cs[(nt0 + j) * 16 + r16];
    float z[2][3][4];
    float p1[2][4], p2[2][4];
#pragma unroll
    for (int m = 0; m < 2; ++m)
#pragma unroll
      for (int r = 0; r < 4; ++r) {
        int rl = (2 * kh + m) * 16 + 4 * g + r;
        f32x2 a = *(const f32x2*)(smem + G1_ST + rl * 8), a2 = *(const f32x2*)(smem + G1_ST + (64 + rl) * 8);
        float mn = (a[0] + a2[0]) * (1.f / D);
        float r1 = __builtin_amdgcn_rsqf(fmaxf((a[1] + a2[1]) * (1.f / D) - mn * mn, 0.f) + 1e-5f);
        float q1 = 0.f, q2 = 0.f;
#pragma unroll
        for (int j = 0; j < 3; ++j) {
          float zz = rbf(gelu_t(fmaf(-mn, cv[j], fin[m][j][r]) * r1));
          z[m][j][r] = zz; q1 += zz; q2 = fmaf(zz, zz, q2);
        }
        p1[m][r] = row16_sum(q1); p2[m][r] = row16_sum(q2);
      }
    GAS u64* zg = (GAS u64*)(ws + OFF_ZG + (size_t)layer * 262144 + (size_t)(p * 2 + kh) * 4096);
    {
      float a = 0.f, c2 = 0.f;
#pragma unroll
      for (int m = 0; m < 2; ++m)
#pragma unroll
        for (int r = 0; r < 4; ++r) if (r16 == m * 4 + r) { a = p1[m][r]; c2 = p2[m][r]; }
      u32 ua = __builtin_bit_cast(u32, a), ub = __builtin_bit_cast(u32, c2);
      if (ub == 0xffffffffu) ub = 0x7fc00000u;
      if (r16 < 8) {
        int rl = (r16 >> 2) * 16 + 4 * g + (r16 & 3);
        __hip_atomic_store(zg + (c * 2 + (w - 2)) * 32 + rl, ((u64)ub << 32) | ua, __ATOMIC_RELAXED, __HIP_MEMORY_SCOPE_AGENT);
      }
    }
    float u1, u2;
    for (int spin = 0;; ++spin) {
      u64 gv[8]; bool ok = true;
#pragma unroll
      for (int i = 0; i < 8; ++i) gv[i] = __hip_atomic_load(zg + ((lane >> 5) * 8 + i) * 32 + (lane & 31), __ATOMIC_RELAXED, __HIP_MEMORY_SCOPE_AGENT);
      u1 = 0.f; u2 = 0.f;
#pragma unroll
      for (int i = 0; i < 8; ++i) {
        ok &= (u32)(gv[i] >> 32) != 0xffffffffu;
        u1 += __builtin_bit_cast(float, (u32)gv[i]); u2 += __builtin_bit_cast(float, (u32)(gv[i] >> 32));
      }
      if (__all(ok) || spin > 200000) break;
      __builtin_amdgcn_s_sleep(1);
    }
    u1 += __shfl_xor(u1, 32); u2 += __shfl_xor(u2, 32);
    float mz = u1 * (1.f / FH);
    float rz = __builtin_amdgcn_rsqf(fmaxf(u2 * (1.f / FH) - mz * mz, 0.f) + 1e-5f);
    float mrz = -mz * rz;
#pragma unroll
    for (int m = 0; m < 2; ++m) {
      float rr[4], mm[4];
#pragma unroll
      for (int r = 0; r < 4; ++r) { rr[r] = __shfl(rz, m * 16 + 4 * g + r); mm[r] = __shfl(mrz, m * 16 + 4 * g + r); }
      int s = p * 64 + (2 * kh + m) * 16 + 4 * g;
#pragma unroll
      for (int j = 0; j < 3; ++j) {
        int ntl = 3 * (w & 1) + j;
        size_t off = ((size_t)(((c * 64 + (s >> 5)) * 6 + ntl) * 64 + ((s & 31) >> 3) * 16 + r16) * 8 + (s & 7)) * 2;
        *(u32x2*)(ws + OFF_Z2F + off) = (u32x2){pack2(fmaf(z[m][j][0], rr[0], mm[0]), fmaf(z[m][j][1], rr[1], mm[1])),
                                                pack2(fmaf(z[m][j][2], rr[2], mm[2]), fmaf(z[m][j][3], rr[3], mm[3]))};
      }
    }
  }
}

constexpr int G2_LDS = 72 * 1024;
__global__ __launch_bounds__(256, 1) void k_gemm2(const u16* __restrict__ bsp, char* __restrict__ ws) {
  extern __shared__ __attribute__((aligned(16))) char smem[];
  const int b = blockIdx.x, tid = threadIdx.x, w = tid >> 6, lane = tid & 63;
  const int xcd = b & 7, idx = b >> 3;
  const int p = (xcd >> 1) * 8 + (idx >> 2), c = (xcd & 1) * 4 + (idx & 3);
  const int r16 = lane & 15, g = lane >> 4;
  const char* ap = ws + OFF_WSPF + ((size_t)(p * 64 + w) * 4 * 64 + lane) * 16;
  const char* bp = ws + OFF_Z2F + ((size_t)(c * 64 + w) * 6 * 64 + lane) * 16;
  f32x4 acc[4][6];
#pragma unroll
  for (int m = 0; m < 4; ++m)
#pragma unroll
    for (int j = 0; j < 6; ++j) acc[m][j] = (f32x4){0, 0, 0, 0};
  constexpr int NB = 6;
  u32x4 A[NB][4], B[NB][6];
#pragma unroll
  for (int i = 0; i < NB - 1; ++i) {
#pragma unroll
    for (int m = 0; m < 4; ++m) A[i][m] = *(const u32x4*)(ap + i * 16384 + m * 1024);
#pragma unroll
    for (int j = 0; j < 6; ++j) B[i][j] = *(const u32x4*)(bp + i * 24576 + j * 1024);
  }
#pragma unroll
  for (int i = 0; i < 16; ++i) {
    const int nx = i + NB - 1;
    if (nx < 16) {
#pragma unroll
      for (int m = 0; m < 4; ++m) A[nx % NB][m] = *(const u32x4*)(ap + nx * 16384 + m * 1024);
#pragma unroll
      for (int j = 0; j < 6; ++j) B[nx % NB][j] = *(const u32x4*)(bp + nx * 24576 + j * 1024);
    }
#pragma unroll
    for (int m = 0; m < 4; ++m)
#pragma unroll
      for (int j = 0; j < 6; ++j) acc[m][j] = mfma(B[i % NB][j], A[i % NB][m], acc[m][j]);
  }
  u32x2 z1[6];
#pragma unroll
  for (int j = 0; j < 6; ++j) {
    int f = c * 96 + j * 16 + 4 * g;
    size_t off = ((size_t)(((p * 24 + (f >> 5)) * 4 + w) * 64 + ((f & 31) >> 3) * 16 + r16) * 8 + (f & 7)) * 2;
    z1[j] = *(const u32x2*)(ws + OFF_Z1F + off);
  }
  const float bt = b2f(bsp[p * 64 + w * 16 + r16]);
#pragma unroll
  for (int m = 0; m < 4; ++m) {
    if (m != w) {
      int slot = (w - m - 1) & 3;
#pragma unroll
      for (int j = 0; j < 6; ++j) *(f32x4*)(smem + (((m * 3 + slot) * 6 + j) * 64 + lane) * 16) = acc[m][j];
    }
  }
  __syncthreads();
#pragma unroll
  for (int j = 0; j < 6; ++j) {
    f32x4 s = w == 0 ? acc[0][j] : w == 1 ? acc[1][j] : w == 2 ? acc[2][j] : acc[3][j];
#pragma unroll
    for (int sl = 0; sl < 3; ++sl) s += *(const f32x4*)(smem + (((w * 3 + sl) * 6 + j) * 64 + lane) * 16);
    int f = c * 96 + j * 16 + 4 * g;
    size_t off = ((size_t)(((p * 24 + (f >> 5)) * 4 + w) * 64 + ((f & 31) >> 3) * 16 + r16) * 8 + (f & 7)) * 2;
    float h0 = blo(z1[j][0]) * (s[0] + bt), h1 = bhi(z1[j][0]) * (s[1] + bt), h2 = blo(z1[j][1]) * (s[2] + bt), h3 = bhi(z1[j][1]) * (s[3] + bt);
    *(u32x2*)(ws + OFF_HF + off) = (u32x2){pack2(h0, h1), pack2(h2, h3)};
  }
}

constexpr int G3_LDS = 60 * 1024;
__global__ __launch_bounds__(256, 1) void k_gemm3(char* __restrict__ ws, u16* __restrict__ out, int last) {
  extern __shared__ __attribute__((aligned(16))) char smem[];
  const int b = blockIdx.x, tid = threadIdx.x, w = tid >> 6, lane = tid & 63;
  const int xcd = b & 7, idx = b >> 3;
  const int p = (xcd >> 1) * 8 + (idx >> 2), c = (xcd & 1) * 4 + (idx & 3);
  const int r16 = lane & 15, g = lane >> 4;
  const char* ap = ws + OFF_HF + ((size_t)(p * 24 + w) * 4 * 64 + lane) * 16;
  const char* bp = ws + OFF_WOUTF + ((size_t)(c * 24 + w) * 5 * 64 + lane) * 16;
  u32x4 A[6][4], B[6][5];
#pragma unroll
  for (int i = 0; i < 6; ++i) {
#pragma unroll
    for (int m = 0; m < 4; ++m) A[i][m] = *(const u32x4*)(ap + i * 16384 + m * 1024);
#pragma unroll
    for (int j = 0; j < 5; ++j) B[i][j] = *(const u32x4*)(bp + i * 20480 + j * 1024);
  }
  u32x2 xr[5];
#pragma unroll
  for (int j = 0; j < 5; ++j) {
    int d = c * 80 + j * 16 + 4 * g;
    size_t off = ((size_t)(((p * 4 + w) * 20 + (d >> 5)) * 64 + ((d & 31) >> 3) * 16 + r16) * 8 + (d & 7)) * 2;
    xr[j] = *(const u32x2*)(ws + OFF_X1F + off);
  }
  f32x4 acc[4][5];
#pragma unroll
  for (int m = 0; m < 4; ++m)
#pragma unroll
    for (int j = 0; j < 5; ++j) acc[m][j] = (f32x4){0, 0, 0, 0};
#pragma unroll
  for (int i = 0; i < 6; ++i)
#pragma unroll
    for (int m = 0; m < 4; ++m)
#pragma unroll
      for (int j = 0; j < 5; ++j) acc[m][j] = mfma(B[i][j], A[i][m], acc[m][j]);
#pragma unroll
  for (int m = 0; m < 4; ++m) {
    if (m != w) {
      int slot = (w - m - 1) & 3;
#pragma unroll
      for (int j = 0; j < 5; ++j) *(f32x4*)(smem + (((m * 3 + slot) * 5 + j) * 64 + lane) * 16) = acc[m][j];
    }
  }
  __syncthreads();
  float s1 = 0.f, s2 = 0.f;
  const int row = p * 64 + w * 16 + r16;
#pragma unroll
  for (int j = 0; j < 5; ++j) {
    f32x4 s = w == 0 ? acc[0][j] : w == 1 ? acc[1][j] : w == 2 ? acc[2][j] : acc[3][j];
#pragma unroll
    for (int sl = 0; sl < 3; ++sl) s += *(const f32x4*)(smem + (((w * 3 + sl) * 5 + j) * 64 + lane) * 16);
    u32x2 o = {pack2(blo(xr[j][0]) + s[0], bhi(xr[j][0]) + s[1]), pack2(blo(xr[j][1]) + s[2], bhi(xr[j][1]) + s[3])};
    int d = c * 80 + j * 16 + 4 * g;
    if (last) {
      *(u32x2*)(out + (size_t)row * D + d) = o;
    } else {
      size_t off = ((size_t)(((p * 4 + w) * 20 + (d >> 5)) * 64 + ((d & 31) >> 3) * 16 + r16) * 8 + (d & 7)) * 2;
      *(u32x2*)(ws + OFF_X2F + off) = o;
      float a0 = blo(o[0]), a1 = bhi(o[0]), a2 = blo(o[1]), a3 = bhi(o[1]);
      s1 += (a0 + a1) + (a2 + a3);
      s2 = fmaf(a0, a0, fmaf(a1, a1, fmaf(a2, a2, fmaf(a3, a3, s2))));
    }
  }
  if (!last) {
    s1 += __shfl_xor(s1, 16); s2 += __shfl_xor(s2, 16);
    s1 += __shfl_xor(s1, 32); s2 += __shfl_xor(s2, 32);
    if (lane < 16) *(f32x2*)(ws + OFF_XST + (size_t)row * 64 + c * 8) = (f32x2){s1, s2};
  }
}

extern "C" void kernel_launch(void* const* d_in, const int* in_sizes, int n_in, void* d_out, int out_size, void* d_ws, size_t ws_size,
                              hipStream_t stream) {
  const u16* q = (const u16*)d_in[0]; const u16* k = (const u16*)d_in[1]; const u16* v = (const u16*)d_in[2];
  const u16* sk = (const u16*)d_in[3]; const u16* hk = (const u16*)d_in[4]; const u16* Dv = (const u16*)d_in[5];
  const u16* win = (const u16*)d_in[7]; const u16* wsp = (const u16*)d_in[8]; const u16* bsp = (const u16*)d_in[9];
  const u16* wout = (const u16*)d_in[10];
  char* ws = (char*)d_ws;
  hipFuncSetAttribute((const void*)k_conv, hipFuncAttributeMaxDynamicSharedMemorySize, CV_LDS);
  hipFuncSetAttribute((const void*)k_gemm1, hipFuncAttributeMaxDynamicSharedMemorySize, G1_LDS);
  hipFuncSetAttribute((const void*)k_gemm2, hipFuncAttributeMaxDynamicSharedMemorySize, G2_LDS);
  hipFuncSetAttribute((const void*)k_gemm3, hipFuncAttributeMaxDynamicSharedMemorySize, G3_LDS);
  k_prep<<<256, 512, 0, stream>>>(q, k, v, sk, win, ws);
  k_conv<<<256, 256, CV_LDS, stream>>>(hk, Dv, wsp, wout, ws);
  for (int l = 0; l < 2; ++l) {
    k_gemm1<<<256, 512, G1_LDS, stream>>>(ws + (l ? OFF_X2F : OFF_X0F), ws + OFF_X1F, ws, l);
    k_gemm2<<<256, 256, G2_LDS, stream>>>(bsp, ws);
    k_gemm3<<<256, 256, G3_LDS, stream>>>(ws, (u16*)d_out, l);
  }
}
```

```cpp
#include <hip/hip_runtime.h>
#include <hip/hip_bf16.h>
typedef unsigned short u16;
typedef unsigned int u32;
typedef unsigned long long u64;
typedef short bf16x8 __attribute__((ext_vector_type(8)));
typedef short s16x4 __attribute__((ext_vector_type(4)));
typedef float f32x4 __attribute__((ext_vector_type(4)));
typedef float f32x2 __attribute__((ext_vector_type(2)));
typedef unsigned u32x4 __attribute__((ext_vector_type(4)));
typedef unsigned u32x2 __attribute__((ext_vector_type(2)));
typedef __bf16 bf16x2_t __attribute__((ext_vector_type(2)));
#define LAS __attribute__((address_space(3)))
#define GAS __attribute__((address_space(1)))
#define DEV __device__ __forceinline__
#define RLX_AGENT __ATOMIC_RELAXED, __HIP_MEMORY_SCOPE_AGENT

constexpr int S = 2048, D = 640, F = 1536, FH = 768;
constexpr int SZX = S * D * 2, SZZ = S * FH * 2;
constexpr int OFF_FL = 0;
constexpr int FL2 = 0, FL3 = 8192, FL1 = 16384;
constexpr int OFF_ZG = 81920;
constexpr int OFF_XST = OFF_ZG + 524288;
constexpr int OFF_CSP = OFF_XST + 524288;
constexpr int OFF_X0F = OFF_CSP + 122880;
constexpr int OFF_X2F = OFF_X0F + SZX;
constexpr int OFF_YF = OFF_X2F + SZX;
constexpr int OFF_WINF = OFF_YF + SZX;
constexpr int OFF_WOUTF = OFF_WINF + D * F * 2;
constexpr int OFF_Z2F = OFF_WOUTF + FH * D * 2;
constexpr int OFF_HF = OFF_Z2F + 2 * SZZ;
constexpr int WS_USED = OFF_HF + 2 * SZZ;

#define STAMP(kid, i)
#define G2NB 4
#define G1SPLIT 4

DEV u32 pack2(float a, float b) { f32x2 v = {a, b}; bf16x2_t r = __builtin_convertvector(v, bf16x2_t); return __builtin_bit_cast(u32, r); }
DEV float blo(u32 x) { return __builtin_bit_cast(float, x << 16); }
DEV float bhi(u32 x) { return __builtin_bit_cast(float, x & 0xffff0000u); }
DEV float b2f(u16 x) { return __builtin_bit_cast(float, (u32)x << 16); }
DEV f32x4 mfma(u32x4 a, u32x4 b, f32x4 c) {
  return __builtin_amdgcn_mfma_f32_16x16x32_bf16(__builtin_bit_cast(bf16x8, a), __builtin_bit_cast(bf16x8, b), c, 0, 0, 0);
}
DEV u32x2 tr_read(const void* p) {
  s16x4 v = __builtin_amdgcn_ds_read_tr16_b64_v4i16((s16x4 LAS*)p);
  return __builtin_bit_cast(u32x2, v);
}
template <int CTRL> DEV float dpp_add(float x) {
  return x + __builtin_bit_cast(float, __builtin_amdgcn_update_dpp(0, __builtin_bit_cast(int, x), CTRL, 0xf, 0xf, false));
}
DEV float dot2(u32 a, u32 b, float c) {
  return __builtin_amdgcn_fdot2_f32_bf16(__builtin_bit_cast(bf16x2_t, a), __builtin_bit_cast(bf16x2_t, b), c, false);
}
DEV float add16(float x) { u32 u = __builtin_bit_cast(u32, x); auto r = __builtin_amdgcn_permlane16_swap(u, u, false, false); return __builtin_bit_cast(float, (u32)r[0]) + __builtin_bit_cast(float, (u32)r[1]); }
DEV float add32(float x) { u32 u = __builtin_bit_cast(u32, x); auto r = __builtin_amdgcn_permlane32_swap(u, u, false, false); return __builtin_bit_cast(float, (u32)r[0]) + __builtin_bit_cast(float, (u32)r[1]); }
DEV float add8(float x) { return dpp_add<0x128>(x); }
template <int V> struct IC { static constexpr int value = V; };
#define SCHED() __builtin_amdgcn_sched_barrier(0)
#define CFENCE() asm volatile("" ::: "memory")
#define DRAIN() do { CFENCE(); __builtin_amdgcn_s_waitcnt(0x0070); CFENCE(); } while (0)
#define LGKM0() do { CFENCE(); __builtin_amdgcn_s_waitcnt(0xC07F); CFENCE(); } while (0)
#define BAR() do { LGKM0(); __builtin_amdgcn_s_barrier(); CFENCE(); } while (0)
#define WAVE_SYNC() do { __builtin_amdgcn_fence(__ATOMIC_RELEASE, "wavefront"); __builtin_amdgcn_wave_barrier(); __builtin_amdgcn_fence(__ATOMIC_ACQUIRE, "wavefront"); } while (0)
typedef __amdgpu_buffer_rsrc_t rsrc_t;
constexpr int SC1 = 16;
DEV u32x4 ld128(rsrc_t rs, int voff, int soff) { return __builtin_amdgcn_raw_buffer_load_b128(rs, voff, soff, SC1); }
DEV u32x4 ld128p(rsrc_t rs, int voff, int soff) { return __builtin_amdgcn_raw_buffer_load_b128(rs, voff, soff, 0); }
DEV void st128(u32x4 v, rsrc_t rs, int voff, int soff) { __builtin_amdgcn_raw_buffer_store_b128(v, rs, voff + soff, 0, SC1); }
DEV void st64(u32x2 v, rsrc_t rs, int voff, int soff) { __builtin_amdgcn_raw_buffer_store_b64(v, rs, voff, soff, SC1); }

constexpr int CV_UR = 4608, CV_H = 4160, CV_WAVE = CV_UR + 4 * CV_H;
constexpr int CV_Y = 4 * CV_WAVE, CV_YS = 4096 + 64;
constexpr int CV_FL = CV_Y + 4 * CV_YS;
constexpr int FRONT_LDS = CV_FL + 64;
#if defined(__HIP_DEVICE_COMPILE__)
#define NOPK __attribute__((target("no-packed-fp32-ops")))
#else
#define NOPK
#endif
__global__ __launch_bounds__(512, 2) NOPK void k_front(const u16* __restrict__ q, const u16* __restrict__ k, const u16* __restrict__ v,
                                                       const u16* __restrict__ sk, const u16* __restrict__ hk, const u16* __restrict__ Dv,
                                                       const u16* __restrict__ win, const u16* __restrict__ wout,
                                                       char* __restrict__ ws) {
  extern __shared__ __attribute__((aligned(16))) char smem[];
  const int b = blockIdx.x, tid = threadIdx.x, lane = tid & 63;
  const int wv = __builtin_amdgcn_readfirstlane(tid >> 6), w = wv & 3;
  const int r16 = lane & 15, g = lane >> 4, l16 = lane * 16;
  const int xcd = b & 7, idx = b >> 3;
  const rsrc_t rs = __builtin_amdgcn_make_buffer_rsrc((void*)ws, 0, WS_USED, 0x00020000);
  STAMP(0, 0);
  if (idx < 20) {
    const int d0 = xcd * 80 + idx * 4;
    volatile LAS u32* rdy = (volatile LAS u32*)(smem + CV_FL);
    if (tid < 8) rdy[tid] = 0u;
    if (wv < 4) {
      const int d = d0 + w;
      char* lur = smem + w * CV_WAVE;
      char* lh = lur + CV_UR;
      float Dd;
      {
        const u16* hp = hk + (size_t)d * S;
        u32x4 hh[4]; u32x2 hn[4];
#pragma unroll
        for (int i = 0; i < 4; ++i) {
          int ci = lane + 64 * i;
          hh[i] = *(const u32x4*)(hp + ci * 8);
          u32x2 tn = *(const u32x2*)(hp + (ci < 255 ? ci * 8 + 8 : ci * 8));
          hn[i][0] = ci < 255 ? tn[0] : 0u; hn[i][1] = ci < 255 ? tn[1] : 0u;
        }
        Dd = b2f(Dv[d]);
        BAR();
        u32x4 z4 = {0, 0, 0, 0};
        if (lane < 32) *(u32x4*)(lur + 4096 + lane * 16) = z4;
        {
          u32 f0 = __builtin_amdgcn_readfirstlane(hh[0][0]), f1 = __builtin_amdgcn_readfirstlane(hh[0][1]);
          if (lane < 8) {
            int qc = lane >> 1, which = lane & 1;
            u32x4 o = z4;
            u32 o2 = qc == 3 ? f0 << 16 : 0u;
            u32 o3 = qc == 1 ? f0 << 16 : qc == 2 ? f0 : qc == 3 ? __builtin_amdgcn_alignbit(f1, f0, 16) : 0u;
            o[2] = which ? o2 : 0u; o[3] = which ? o3 : 0u;
            *(u32x4*)(lh + qc * CV_H + which * 16) = o;
          }
        }
#pragma unroll
        for (int i = 0; i < 4; ++i) {
          int ci = lane + 64 * i;
          u32 w0 = hh[i][0], w1 = hh[i][1], w2 = hh[i][2], w3 = hh[i][3], w4 = hn[i][0], w5 = hn[i][1];
          u32 a01 = __builtin_amdgcn_alignbit(w1, w0, 16), a12 = __builtin_amdgcn_alignbit(w2, w1, 16), a23 = __builtin_amdgcn_alignbit(w3, w2, 16),
              a34 = __builtin_amdgcn_alignbit(w4, w3, 16), a45 = __builtin_amdgcn_alignbit(w5, w4, 16);
          u32x4 c0 = {w0, w1, w2, w3}, c1 = {a01, a12, a23, a34}, c2 = {w1, w2, w3, w4}, c3 = {a12, a23, a34, a45};
          *(u32x4*)(lh + 0 * CV_H + 32 + ci * 16) = c0;
          *(u32x4*)(lh + 1 * CV_H + 32 + ci * 16) = c1;
          *(u32x4*)(lh + 2 * CV_H + 32 + ci * 16) = c2;
          *(u32x4*)(lh + 3 * CV_H + 32 + ci * 16) = c3;
        }
      }
      WAVE_SYNC();
      STAMP(0, 1);
      const int n = r16;
      const int qc = (1 + n) & 3;
      const char* abase = lh + qc * CV_H + (1 + n + 8 * g - qc) * 2;
      const char* bbase = lur + (2032 - 16 * n + 8 * g) * 2;
      f32x4 acc[8];
#pragma unroll
      for (int t = 0; t < 8; ++t) acc[t] = (f32x4){0, 0, 0, 0};
      u32x4 PF[32], B0[8], B1[8];
#define LDA_CV(cc) ({ u32x2 lo_ = *(const volatile LAS u32x2*)(abase + (cc) * 64), hi_ = *(const volatile LAS u32x2*)(abase + (cc) * 64 + 8); (u32x4){lo_[0], lo_[1], hi_[0], hi_[1]}; })
#define CLA(slot, C) do { _Pragma("unroll") for (int i_ = 0; i_ < 8; ++i_) PF[(slot) * 8 + i_] = LDA_CV((C) * 8 + i_); } while (0)
#define CLB(Bx, dl) do { _Pragma("unroll") for (int i_ = 0; i_ < 8; ++i_) Bx[i_] = *(const u32x4*)(bbase - (8 * (dl) - i_) * 64); } while (0)
#define CWAIT(dl) do { while (rdy[dl] == 0u) __builtin_amdgcn_s_sleep(1); CFENCE(); } while (0)
#define CMM(slot, Bx, t) do { SCHED(); _Pragma("unroll") for (int i_ = 0; i_ < 8; ++i_) acc[t] = mfma(PF[(slot) * 8 + i_], Bx[i_], acc[t]); SCHED(); } while (0)
#define CMI(slot, Bx, t, LOADS, ND) do { SCHED(); LOADS; _Pragma("unroll") for (int i_ = 0; i_ < 8; ++i_) acc[t] = mfma(PF[(slot) * 8 + i_], Bx[i_], acc[t]); \
        _Pragma("unroll") for (int i_ = 0; i_ < 8; ++i_) { __builtin_amdgcn_sched_group_barrier(0x8, 1, 0); __builtin_amdgcn_sched_group_barrier(0x100, ND, 0); } SCHED(); } while (0)
      CLA(0, 0); CLA(1, 1); CLA(2, 2); CLA(3, 3);
      CWAIT(0); CLB(B0, 0); CMM(0, B0, 0); CMM(1, B0, 1); CMM(2, B0, 2);
      CWAIT(1); CMI(3, B0, 3, CLB(B1, 1), 1); CMM(0, B1, 1); CMM(1, B1, 2); CMM(2, B1, 3);
      CWAIT(2); CMI(3, B1, 4, CLB(B0, 2), 1); CMM(0, B0, 2); CMM(1, B0, 3); CMM(2, B0, 4);
      CWAIT(3); CMI(3, B0, 5, CLB(B1, 3), 1);
      CMM(3, B1, 6); CMI(2, B1, 5, CLA(3, 4), 2); CMI(1, B1, 4, CLA(2, 5), 2); CMI(0, B1, 3, CLA(1, 6), 2);
      CMI(3, B1, 7, CLA(0, 7), 2); CMI(3, B0, 6, CLB(B1, 1), 1); CMM(2, B0, 7);
      CMI(3, B1, 5, CLB(B0, 0), 1); CMM(2, B1, 6); CMM(1, B1, 7);
      CMM(0, B0, 7); CMI(1, B0, 6, CLA(0, 0), 2); CMI(2, B0, 5, CLA(1, 1), 2);
      CWAIT(4); CMI(3, B0, 4, CLA(2, 2); CLB(B1, 4), 3);
      CMI(0, B1, 4, CLA(3, 3), 2); CMM(1, B1, 5); CMM(2, B1, 6);
      CWAIT(5); CMI(3, B1, 7, CLB(B0, 5), 1); CMM(0, B0, 5); CMM(1, B0, 6);
      CWAIT(6); CMI(2, B0, 7, CLB(B1, 6), 1); CMM(0, B1, 6);
      CWAIT(7); CMI(1, B1, 7, CLB(B0, 7), 1); CMM(0, B0, 7);
      STAMP(0, 2);
      char* ly = smem + CV_Y;
#pragma unroll
      for (int t = 0; t < 8; ++t) {
        int sb = 256 * t + 16 * n + 4 * g;
        u32x2 u = *(const u32x2*)(lur + (2044 - sb) * 2);
        float u3 = blo(u[0]), u2 = bhi(u[0]), u1 = blo(u[1]), u0 = bhi(u[1]);
        u32x2 o = {pack2(fmaf(u0, Dd, acc[t][0]), fmaf(u1, Dd, acc[t][1])), pack2(fmaf(u2, Dd, acc[t][2]), fmaf(u3, Dd, acc[t][3]))};
        *(u32x2*)(ly + w * CV_YS + sb * 2) = o;
      }
    } else {
      u32x4 w0 = *(const u32x4*)(sk + d0 * 4), w1 = *(const u32x4*)(sk + d0 * 4 + 8);
      BAR();
#pragma unroll
      for (int r = 0; r < 2; ++r) {
        if (r == 0) __builtin_amdgcn_s_setprio(2); else __builtin_amdgcn_s_setprio(0);
        const int blk = 4 * r + w, s0 = blk * 256 + lane * 4;
        u32x2 kk[7], vv[4];
#pragma unroll
        for (int i = 0; i < 3; ++i) {
          int s = blk * 256 - 3 + i;
          u32x2 t = *(const u32x2*)(k + (size_t)(s < 0 ? 0 : s) * D + d0);
          kk[i][0] = s < 0 ? 0u : t[0]; kk[i][1] = s < 0 ? 0u : t[1];
        }
#pragma unroll
        for (int i = 0; i < 4; ++i) {
          kk[3 + i] = *(const u32x2*)(k + (size_t)(s0 + i) * D + d0);
          vv[i] = *(const u32x2*)(v + (size_t)(s0 + i) * D + d0);
        }
        SCHED();
#pragma unroll
        for (int i = 0; i < 3; ++i) {
          kk[i][0] = __builtin_amdgcn_update_dpp(kk[i][0], kk[4 + i][0], 0x138, 0xf, 0xf, false);
          kk[i][1] = __builtin_amdgcn_update_dpp(kk[i][1], kk[4 + i][1], 0x138, 0xf, 0xf, false);
        }
#pragma unroll
        for (int ch = 0; ch < 4; ++ch) {
          u32 wa = ch < 2 ? w0[(ch & 1) * 2] : w1[(ch & 1) * 2], wb = ch < 2 ? w0[(ch & 1) * 2 + 1] : w1[(ch & 1) * 2 + 1];
          float c0 = blo(wa), c1 = bhi(wa), c2 = blo(wb), c3 = bhi(wb);
          float kf[7];
#pragma unroll
          for (int i = 0; i < 7; ++i) kf[i] = (ch & 1) ? bhi(kk[i][ch >> 1]) : blo(kk[i][ch >> 1]);
          float o[4];
#pragma unroll
          for (int rr = 0; rr < 4; ++rr) {
            float ks = kf[rr] * c3;
            ks = fmaf(kf[rr + 1], c2, ks); ks = fmaf(kf[rr + 2], c1, ks); ks = fmaf(kf[rr + 3], c0, ks);
            float vf = (ch & 1) ? bhi(vv[rr][ch >> 1]) : blo(vv[rr][ch >> 1]);
            o[rr] = ks * vf;
          }
          *(u32x2*)(smem + ch * CV_WAVE + (2044 - s0) * 2) = (u32x2){pack2(o[3], o[2]), pack2(o[1], o[0])};
        }
        CFENCE();
        rdy[blk] = 1u;
        STAMP(0, 3 + r);
      }
    }
    BAR();
    {
      const char* ly = smem + CV_Y;
      const int qq = r16 >> 2, pp = lane & 3;
      const int ks = d0 >> 5, gg = (d0 & 31) >> 3, j = d0 & 7;
#pragma unroll
      for (int i = 0; i < 4; ++i) {
        int sbk = (wv * 4 + i) * 4 + g;
        u32x2 o = tr_read(ly + qq * CV_YS + (16 * sbk + 4 * pp) * 2);
        *(u32x2*)(ws + OFF_YF + (((sbk * 20 + ks) * 64 + gg * 16 + r16) * 8 + j) * 2) = o;
      }
    }
  } else {
    const int wg = ((idx - 20) * 8 + xcd) * 8 + wv;
    {
      const int id = min(((idx - 20) * 8 + wv) * 64 + lane, 4095);
      const u16* src = ((id & 2) ? v : k) + (size_t)(id >> 2) * D + ((xcd * 160) >> 7) * 64 + (id & 1) * 64;
      u32 t0 = *(const u32*)src, t1 = *(const u32*)(src + (size_t)1024 * D);
      asm volatile("" :: "v"(t0), "v"(t1));
    }
    u16* lw = (u16*)(smem + wv * 4608);
    const int qq = r16 >> 2, pp = lane & 3;
    if (wg < 256) {
      const int row = wg * 8 + (lane & 7), cc = lane >> 3;
      u32x4 x[10];
#pragma unroll
      for (int i = 0; i < 10; ++i) x[i] = *(const u32x4*)(q + (size_t)row * D + (cc + 8 * i) * 8);
      float s1 = 0.f, s2 = 0.f;
#pragma unroll
      for (int i = 0; i < 10; ++i) {
#pragma unroll
        for (int e = 0; e < 4; ++e) { float a = blo(x[i][e]), c = bhi(x[i][e]); s1 += a + c; s2 = fmaf(a, a, fmaf(c, c, s2)); }
        int c = cc + 8 * i, ks = c >> 2, gg = c & 3;
        st128(x[i], rs, (((row >> 4) * 20 + ks) * 64 + gg * 16 + (row & 15)) * 16, OFF_X0F);
      }
      s1 = add8(s1); s2 = add8(s2);
      s1 = add16(s1); s2 = add16(s2);
      s1 = add32(s1); s2 = add32(s2);
      f32x4 st = {cc == 0 ? s1 : 0.f, cc == 0 ? s2 : 0.f, 0.f, 0.f};
      *(f32x4*)(ws + OFF_XST + (size_t)row * 128 + cc * 16) = st;
    } else if (wg < 736) {
      const int tl = wg - 256, ks = tl / 24, n0 = (tl % 24) * 64;
      u32x4 t2[4];
#pragma unroll
      for (int i = 0; i < 4; ++i) t2[i] = *(const u32x4*)(win + (size_t)(ks * 32 + i * 8 + (lane >> 3)) * F + n0 + (lane & 7) * 8);
#pragma unroll
      for (int i = 0; i < 4; ++i) *(u32x4*)(lw + (i * 8 + (lane >> 3)) * 72 + (lane & 7) * 8) = t2[i];
      WAVE_SYNC();
#pragma unroll
      for (int nt = 0; nt < 4; ++nt) {
        u32x2 a = tr_read(lw + (8 * g + qq) * 72 + nt * 16 + 4 * pp);
        u32x2 c = tr_read(lw + (8 * g + 4 + qq) * 72 + nt * 16 + 4 * pp);
        int ntg = (n0 >> 4) + nt;
        st128((u32x4){a[0], a[1], c[0], c[1]}, rs, l16, OFF_WINF + (ntg * 20 + ks) * 1024);
        float cs = blo(a[0]) + bhi(a[0]) + blo(a[1]) + bhi(a[1]) + blo(c[0]) + bhi(c[0]) + blo(c[1]) + bhi(c[1]);
        cs = add16(cs); cs = add32(cs);
        if (lane < 16) *(float*)(ws + OFF_CSP + ((size_t)ks * F + ntg * 16 + lane) * 4) = cs;
      }
      WAVE_SYNC();
    }
    if (wg >= 528) {
      const int tl = wg - 528, ks = tl / 10, n0 = (tl % 10) * 64;
      u32x4 t2[4];
#pragma unroll
      for (int i = 0; i < 4; ++i) t2[i] = *(const u32x4*)(wout + (size_t)(ks * 32 + i * 8 + (lane >> 3)) * D + n0 + (lane & 7) * 8);
#pragma unroll
      for (int i = 0; i < 4; ++i) *(u32x4*)(lw + (i * 8 + (lane >> 3)) * 72 + (lane & 7) * 8) = t2[i];
      WAVE_SYNC();
#pragma unroll
      for (int nt = 0; nt < 4; ++nt) {
        u32x2 a = tr_read(lw + (8 * g + qq) * 72 + nt * 16 + 4 * pp);
        u32x2 c2 = tr_read(lw + (8 * g + 4 + qq) * 72 + nt * 16 + 4 * pp);
        int ntg = (n0 >> 4) + nt;
        st128((u32x4){a[0], a[1], c2[0], c2[1]}, rs, l16, OFF_WOUTF + (((ntg / 5) * 24 + ks) * 5 + ntg % 5) * 1024);
      }
    }
    if (wg < 512) st128((u32x4){0xffffffffu, 0xffffffffu, 0xffffffffu, 0xffffffffu}, rs, l16, OFF_ZG + wg * 1024);
    else if (wg < 592) st128((u32x4){0u, 0u, 0u, 0u}, rs, l16, OFF_FL + (wg - 512) * 1024);
  }
  STAMP(0, 15);
}

constexpr int L_Z1 = 139264;
constexpr int L_X1 = L_Z1 + 12288;
constexpr int L_CS = L_X1 + 10240;
constexpr int L_ST = L_CS + 768;
constexpr int L_SY = L_ST + 1024;
constexpr int LDS_TOTAL = 163840;

DEV float gelu_t(float a) {
  const float k0 = -2.f * 0.7978845608028654f * 1.4426950408889634f;
  float t = a * a;
  float e = __builtin_amdgcn_exp2f(a * fmaf(t, k0 * 0.044715f, k0));
  return a * __builtin_amdgcn_rcpf(1.f + e);
}

DEV void poll_flags(const char* ws, int off, int n, u32 want, int lane) {
  GAS u32* f = (GAS u32*)(ws + off) + (lane < n ? lane : 0);
  __builtin_amdgcn_s_setprio(3);
#pragma unroll 1
  for (int spin = 0; spin < 400000; ++spin) {
    u32 v = __hip_atomic_load(f, RLX_AGENT);
    if (__all(v == want)) break;
    __builtin_amdgcn_s_sleep(1);
  }
  __builtin_amdgcn_s_setprio(0);
}

__global__ __launch_bounds__(512, 2) NOPK void k_main(const u16* __restrict__ wsp, const u16* __restrict__ bsp, char* __restrict__ ws,
                                                 u16* __restrict__ out) {
  extern __shared__ __attribute__((aligned(16))) char smem[];
  const int b = blockIdx.x, tid = threadIdx.x, lane0 = tid & 63;
  const int wv0 = __builtin_amdgcn_readfirstlane(tid >> 6);
  const rsrc_t rs = __builtin_amdgcn_make_buffer_rsrc((void*)ws, 0, WS_USED, 0x00020000);
  const rsrc_t rw = __builtin_amdgcn_make_buffer_rsrc((void*)wsp, 0, S * S * 2, 0x00020000);
  u32x4 PF[32];
  u32x4 XY[10];
#define WDMA(gen, u) do { const int u_ = (u), kq_ = u_ >> 5; \
    __builtin_amdgcn_raw_ptr_buffer_load_lds(rw, (LAS void*)(smem + u_ * 1024), 16, (lane >> 3) * 4096 + (((lane & 7) ^ (lane >> 3)) << 4), \
        (p * 64 + (((((u_ & 7) >> 1) + kq_) & 3) << 4) + ((u_ & 1) << 3)) * 4096 + (kq_ + 16 * (gen) + 4 * ((u_ >> 3) & 3)) * 128, 0, 0); } while (0)
#define WFRAG(i, m) *(const u32x4*)(((i) & 1 ? wa1 : wa0) + ((i) >> 1) * 8192 + (m) * 2048)
#define WADDR() const char* wa0 = smem + w * 32768 + r16 * 128 + ((g ^ (r16 & 7)) << 4); const char* wa1 = smem + w * 32768 + r16 * 128 + (((4 + g) ^ (r16 & 7)) << 4)
#define LAST_OF(word, n) ({ u32 old_ = 0; if (lane == 0) old_ = __hip_atomic_fetch_add((LAS u32*)(smem + L_SY + 4 * (word)), 1u, __ATOMIC_RELAXED, __HIP_MEMORY_SCOPE_WORKGROUP); \
    __builtin_amdgcn_readfirstlane(old_) == (u32)(n) - 1u; })
#define VM0() do { CFENCE(); __builtin_amdgcn_s_waitcnt(0x0070); CFENCE(); } while (0)
#define IDS() \
  int b_ = b, wv_ = wv0, ln_ = lane0; asm volatile("" : "+s"(b_)); asm volatile("" : "+s"(wv_)); asm volatile("" : "+v"(ln_)); \
  const int wv = wv_, lane = ln_; \
  const int p = ((b_ & 7) >> 1) * 8 + (b_ >> 5), c = (b_ & 1) * 4 + ((b_ >> 3) & 3); \
  const int r16 = lane & 15, g = lane >> 4, w = wv & 3, hi = wv >> 2, l16 = lane * 16, cb = wv & 1, kh = (wv >> 1) & 1; (void)cb; (void)kh; \
  const int e8 = (g >> 1) * 256 + r16 * 16 + (g & 1) * 8; \
  const int aoff = ((p * 4 + w) * 20 + hi * 10) * 1024; \
  const int boff = OFF_WINF + ((48 * hi + 6 * c + 3 * cb) * 20 + kh * 10) * 1024; \
  const int wooff = OFF_WOUTF + (c * 24 + wv) * 5120; \
  (void)r16; (void)g; (void)e8; (void)aoff; (void)boff; (void)wooff; (void)l16; (void)hi; (void)p; (void)c;
#define G1_PF_XY() do { \
    _Pragma("unroll") for (int i_ = 0; i_ < 10; ++i_) XY[i_] = ld128p(rs, l16 + (i_ & 3) * 1024, OFF_YF + aoff + (i_ >> 2) * 4096); \
    SCHED(); } while (0)
#define G1_PF_W(i0, i1) do { \
    _Pragma("unroll") for (int i_ = (i0); i_ < (i1); ++i_) _Pragma("unroll") for (int j_ = 0; j_ < 3; ++j_) PF[i_ * 3 + j_] = ld128p(rs, l16 + (i_ & 3) * 1024, boff + j_ * 20480 + (i_ >> 2) * 4096); \
    SCHED(); } while (0)
  STAMP(1, 0);
  {
    IDS();
    if (tid < 16) *(u32*)(smem + L_SY + tid * 4) = 0u;
  }
  STAMP(1, 15);

  auto layer = [&](auto LC) __attribute__((always_inline)) {
    constexpr int l = decltype(LC)::value;
    f32x4 acc[4][3];
    u32 tch = 0;
    {
    IDS();
    STAMP(2 + 3 * l, 0);
    STAMP(2 + 3 * l, 1);
    {
      u32x4 PO[10];
      const int pooff = (l ? OFF_X2F : OFF_X0F) + aoff;
      u32x4 stq = ld128(rs, r16 * 128 + g * 32, OFF_XST + l * 262144 + (p * 64 + w * 16) * 128);
      u32x4 stq2 = ld128(rs, r16 * 128 + g * 32 + 16, OFF_XST + l * 262144 + (p * 64 + w * 16) * 128);
#pragma unroll
      for (int i = 0; i < 10; ++i) {
        PO[i] = ld128(rs, l16 + (i & 3) * 1024, pooff + (i >> 2) * 4096);
        if (l == 0) XY[i] = ld128p(rs, l16 + (i & 3) * 1024, OFF_YF + aoff + (i >> 2) * 4096);
      }
      SCHED();
      f32x4 stp = __builtin_bit_cast(f32x4, stq) + __builtin_bit_cast(f32x4, stq2);
      float s1 = stp[0] + stp[2], s2 = stp[1] + stp[3];
      s1 = add16(s1); s2 = add16(s2);
      s1 = add32(s1); s2 = add32(s2);
      const float mean = s1 * (1.f / D);
      const float rsd = __builtin_amdgcn_rsqf(fmaxf(s2 * (1.f / D) - mean * mean, 0.f) + 1e-5f);
      const float mr = -mean * rsd;
      float t1 = 0.f, t2 = 0.f;
#pragma unroll
      for (int i = 0; i < 10; ++i) {
        u32x4 o;
        if (l == 0 && i == 3) G1_PF_W(0, G1SPLIT);
        if (i == 8) G1_PF_W(G1SPLIT, 7);
#pragma unroll
        for (int e = 0; e < 4; ++e) {
          u32 xw = PO[i][e], yw = XY[i][e];
          float xa = blo(xw), xb = bhi(xw), ya = blo(yw), yb = bhi(yw);
          float oa = fmaf(fmaf(xa, rsd, mr), ya, xa), ob = fmaf(fmaf(xb, rsd, mr), yb, xb);
          u32 pk = pack2(oa, ob);
          o[e] = pk;
          t1 = dot2(pk, 0x3f803f80u, t1); t2 = dot2(pk, pk, t2);
        }
        *(u32x4*)(smem + ((hi * 10 + i) * 4 + w) * 1024 + lane * 16) = o;
      }
      t1 = add16(t1); t2 = add16(t2);
      t1 = add32(t1); t2 = add32(t2);
      if (lane < 16) *(f32x2*)(smem + L_ST + (hi * 64 + w * 16 + lane) * 8) = (f32x2){t1, t2};
    }
    STAMP(2 + 3 * l, 2);
    BAR();
    STAMP(2 + 3 * l, 3);
    if (wv < 4) {
#pragma unroll
      for (int j = 0; j < 5; ++j) {
        const int nt = c * 5 + j;
        u32x2 t = *(const u32x2*)(smem + ((nt >> 1) * 4 + w) * 1024 + (nt & 1) * 512 + e8);
        *(u32x2*)(smem + L_X1 + ((w * 5 + j) * 64 + lane) * 8) = t;
      }
    }
#pragma unroll
    for (int m = 0; m < 4; ++m)
#pragma unroll
      for (int j = 0; j < 3; ++j) acc[m][j] = (f32x4){0, 0, 0, 0};
    {
      u32x4 A[2][4];
      float cst[10];
      const char* ab0 = smem + kh * 40960 + kh * 2048 + l16;
      const char* ab1 = smem + kh * 40960 + (1 - kh) * 2048 + l16;
#define G1A(i, m) *(const u32x4*)(((m) < 2 ? ab0 : ab1) + (i) * 4096 + ((m) & 1) * 1024)
#pragma unroll
      for (int m = 0; m < 4; ++m) A[0][m] = G1A(0, m);
#pragma unroll
      for (int i = 0; i < 10; ++i) {
        if (i < 9) {
#pragma unroll
          for (int m = 0; m < 4; ++m) A[(i + 1) & 1][m] = G1A(i + 1, m);
        }
        SCHED();
#pragma unroll
        for (int m = 0; m < 4; ++m)
#pragma unroll
          for (int j = 0; j < 3; ++j) acc[m][j] = mfma(PF[i * 3 + j], A[i & 1][m], acc[m][j]);
        SCHED();
        if (i < 3) G1_PF_W(7 + i, 8 + i);
        if (l == 0 && i == 3) {
          const int li = tid % 192, col = (li < 96 ? 0 : 768 - 96) + 96 * c + li;
          const float* csp = (const float*)(ws + OFF_CSP) + (tid >= 192 ? 10 * F : 0) + col;
#pragma unroll
          for (int q_ = 0; q_ < 10; ++q_) cst[q_] = csp[q_ * F];
          SCHED();
        }
        if (l == 0 && i == 3) {
          tch = __builtin_amdgcn_raw_buffer_load_b32(rw, (wv * 32 + (lane & 31)) * 128, (p * 64 + c * 8) * 4096, 0);
          SCHED();
        }
        if (l == 0 && i == 6) {
          float sum = 0.f;
#pragma unroll
          for (int q_ = 0; q_ < 10; ++q_) sum += cst[q_];
          if (tid < 384) *(float*)(smem + 131072 + tid * 4) = sum;
        }
        if (i >= 4 && i < 10) WDMA(0, 80 + wv * 6 + (i - 4));
      }
      SCHED();
    }
    STAMP(2 + 3 * l, 4);
    BAR();
    if (l == 0 && tid < 192) *(float*)(smem + L_CS + tid * 4) = *(const float*)(smem + 131072 + tid * 4) + *(const float*)(smem + 131072 + 768 + tid * 4);
    WDMA(0, 48 + wv * 4); WDMA(0, 49 + wv * 4); WDMA(0, 50 + wv * 4); WDMA(0, 51 + wv * 4);
#pragma unroll
    for (int m = 0; m < 2; ++m)
#pragma unroll
      for (int j = 0; j < 3; ++j) {
        f32x4 give = acc[2 + m][j];
        *(f32x4*)(smem + ((wv * 6 + m * 3 + j) * 64 + lane) * 16) = give;
      }
    BAR();
    f32x4 fin[2][3];
#pragma unroll
    for (int m = 0; m < 2; ++m)
#pragma unroll
      for (int j = 0; j < 3; ++j) {
        f32x4 got = *(const f32x4*)(smem + (((wv ^ 2) * 6 + m * 3 + j) * 64 + lane) * 16);
        f32x4 keep = acc[m][j];
        fin[m][j] = keep + got;
      }
    BAR();
    STAMP(2 + 3 * l, 5);
#define WOUT_PREFETCH() do { _Pragma("unroll") for (int i = 0; i < 3; ++i) _Pragma("unroll") for (int j = 0; j < 5; ++j) \
      PF[16 + i * 5 + j] = ld128(rs, l16 + (j & 3) * 1024, wooff + i * 40960 + (j >> 2) * 4096); SCHED(); } while (0)
    if (hi) __builtin_amdgcn_s_setprio(3);
    else {
#pragma unroll
      for (int i = 0; i < 12; ++i) WDMA(0, w * 12 + i);
    }
    volatile LAS u32* sy = (volatile LAS u32*)(smem + L_SY);
    WADDR();
#define GEN1_TO_REGS() do { asm volatile("" :: "v"(tch)); while (sy[0] < 8u * (l + 1)) __builtin_amdgcn_s_sleep(1); CFENCE(); \
      _Pragma("unroll") for (int f_ = 0; f_ < 32; ++f_) PF[f_] = WFRAG(f_ >> 2, f_ & 3); SCHED(); CFENCE(); \
      if (lane == 0) __hip_atomic_fetch_add((LAS u32*)(smem + L_SY + 8), 1u, __ATOMIC_RELAXED, __HIP_MEMORY_SCOPE_WORKGROUP); } while (0)
#define GEN2_DMA() do { while (sy[2] < 8u * (l + 1)) __builtin_amdgcn_s_sleep(1); CFENCE(); \
      _Pragma("unroll") for (int i_ = 0; i_ < 32; ++i_) WDMA(1, w * 32 + i_); SCHED(); } while (0)
    SCHED();
    {
      const float* cs = (const float*)(smem + L_CS) + (hi * 2 + cb) * 48 + 4 * g;
      float z[2][3][4];
      float q1[2], q2[2];
#pragma unroll
      for (int m = 0; m < 2; ++m) {
        int rl = (2 * kh + m) * 16 + r16;
        f32x2 a = *(const f32x2*)(smem + L_ST + rl * 8), a2 = *(const f32x2*)(smem + L_ST + (64 + rl) * 8);
        float mn = (a[0] + a2[0]) * (1.f / D);
        float r1 = __builtin_amdgcn_rsqf(fmaxf((a[1] + a2[1]) * (1.f / D) - mn * mn, 0.f) + 1e-5f);
        q1[m] = 0.f; q2[m] = 0.f;
#pragma unroll
        for (int j = 0; j < 3; ++j) {
          f32x4 cv = *(const f32x4*)(cs + j * 16);
#pragma unroll
          for (int r = 0; r < 4; ++r) {
            float zz = gelu_t(fmaf(-mn, cv[r], fin[m][j][r]) * r1);
            z[m][j][r] = zz; q1[m] += zz; q2[m] = fmaf(zz, zz, q2[m]);
          }
        }
      }
      if (!hi) {
#pragma unroll
        for (int m = 0; m < 2; ++m)
#pragma unroll
          for (int j = 0; j < 3; ++j)
            *(u32x2*)(smem + L_Z1 + (((2 * kh + m) * 6 + 3 * cb + j) * 64 + lane) * 8) =
                (u32x2){pack2(z[m][j][0], z[m][j][1]), pack2(z[m][j][2], z[m][j][3])};
        VM0();
        if (lane == 0) __hip_atomic_fetch_add((LAS u32*)(smem + L_SY), 1u, __ATOMIC_RELAXED, __HIP_MEMORY_SCOPE_WORKGROUP);
        GEN1_TO_REGS();
        GEN2_DMA();
        BAR();
      } else {
#pragma unroll
        for (int m = 0; m < 2; ++m) {
          q1[m] = add16(q1[m]); q2[m] = add16(q2[m]);
          q1[m] = add32(q1[m]); q2[m] = add32(q2[m]);
        }
        GAS u64* zg = (GAS u64*)(ws + OFF_ZG + l * 262144 + (p * 2 + kh) * 4096);
        VM0();
        if (lane == 0) __hip_atomic_fetch_add((LAS u32*)(smem + L_SY), 1u, __ATOMIC_RELAXED, __HIP_MEMORY_SCOPE_WORKGROUP);
        {
          float a = (g & 1) ? q1[1] : q1[0], c2 = (g & 1) ? q2[1] : q2[0];
          u32 ua = __builtin_bit_cast(u32, a), ub = __builtin_bit_cast(u32, c2);
          if (ub == 0xffffffffu) ub = 0x7fc00000u;
          if (lane < 32) __hip_atomic_store(zg + (c * 2 + cb) * 32 + lane, ((u64)ub << 32) | ua, RLX_AGENT);
        }
        STAMP(2 + 3 * l, 6);
        GEN1_TO_REGS();
        float u1[2], u2[2];
#pragma unroll 1
        for (int spin = 0;; ++spin) {
          u64 gv[2][4]; bool ok = true;
#pragma unroll
          for (int m = 0; m < 2; ++m)
#pragma unroll
            for (int i = 0; i < 4; ++i) gv[m][i] = __hip_atomic_load(zg + (g * 4 + i) * 32 + m * 16 + r16, RLX_AGENT);
#pragma unroll
          for (int m = 0; m < 2; ++m) {
            u1[m] = 0.f; u2[m] = 0.f;
#pragma unroll
            for (int i = 0; i < 4; ++i) {
              ok &= (u32)(gv[m][i] >> 32) != 0xffffffffu;
              u1[m] += __builtin_bit_cast(float, (u32)gv[m][i]); u2[m] += __builtin_bit_cast(float, (u32)(gv[m][i] >> 32));
            }
          }
          if (__all(ok) || spin > 200000) break;
          __builtin_amdgcn_s_sleep(1);
        }
        STAMP(2 + 3 * l, 7);
        char* lt = smem + 131072 + w * 2048;
        float rz[2], mrz[2];
#pragma unroll
        for (int m = 0; m < 2; ++m) {
          u1[m] = add16(u1[m]); u2[m] = add16(u2[m]);
          u1[m] = add32(u1[m]); u2[m] = add32(u2[m]);
          float mz = u1[m] * (1.f / FH);
          rz[m] = __builtin_amdgcn_rsqf(fmaxf(u2[m] * (1.f / FH) - mz * mz, 0.f) + 1e-5f);
          mrz[m] = -mz * rz[m];
        }
        const int qq = r16 >> 2, pp = lane & 3;
#pragma unroll
        for (int j = 0; j < 3; ++j) {
#pragma unroll
          for (int m = 0; m < 2; ++m)
            *(u32x2*)(lt + (m * 16 + r16) * 40 + 8 * g) =
                (u32x2){pack2(fmaf(z[m][j][0], rz[m], mrz[m]), fmaf(z[m][j][1], rz[m], mrz[m])), pack2(fmaf(z[m][j][2], rz[m], mrz[m]), fmaf(z[m][j][3], rz[m], mrz[m]))};
          WAVE_SYNC();
          u32x2 a = tr_read(lt + (8 * g + qq) * 40 + 8 * pp);
          u32x2 c2 = tr_read(lt + (8 * g + 4 + qq) * 40 + 8 * pp);
          WAVE_SYNC();
          st128((u32x4){a[0], a[1], c2[0], c2[1]}, rs, l16 + j * 1024, OFF_Z2F + l * SZZ + ((c * 64 + p * 2 + kh) * 6 + 3 * cb) * 1024);
        }
        DRAIN();
        __builtin_amdgcn_s_setprio(0);
        BAR();
        if (wv == 5 && lane < 32) __hip_atomic_store((GAS u32*)(ws + OFF_FL + FL1 + (l * 8 + c) * 4096 + lane * 128) + p, 1u, RLX_AGENT);
        if (wv == 4) poll_flags(ws, OFF_FL + FL1 + (l * 8 + c) * 4096 + p * 128, 32, 1u, lane);
      }
    }
    STAMP(2 + 3 * l, 15);
    }
    {
    IDS();
    STAMP(3 + 3 * l, 0);
    const float bt = b2f(bsp[p * 64 + w * 16 + r16]);
    BAR();
    STAMP(3 + 3 * l, 1);
    {
      const int zoff = OFF_Z2F + l * SZZ + ((c * 64 + 2 * w) * 6 + 3 * hi) * 1024;
#define ZOFS(i) (((i) >> 1) * 49152 + ((i) & 1) * 6144)
      constexpr int NB = G2NB;
      u32x4 B[NB][3];
#pragma unroll
      for (int i = 0; i < NB - 1; ++i)
#pragma unroll
        for (int j = 0; j < 3; ++j) B[i][j] = ld128(rs, l16 + j * 1024, zoff + ZOFS(i));
      SCHED();
#pragma unroll
      for (int m = 0; m < 4; ++m)
#pragma unroll
        for (int j = 0; j < 3; ++j) acc[m][j] = (f32x4){0, 0, 0, 0};
      WADDR();
      volatile LAS u32* sy = (volatile LAS u32*)(smem + L_SY);
#pragma unroll
      for (int i = 0; i < 16; ++i) {
        const int nx = i + NB - 1;
        if (nx < 16) {
#pragma unroll
          for (int j = 0; j < 3; ++j) B[nx % NB][j] = ld128(rs, l16 + j * 1024, zoff + ZOFS(nx));
        }
        if (i == 1) {
          CFENCE();
          if (lane == 0) __hip_atomic_fetch_add((LAS u32*)(smem + L_SY + 4), 1u, __ATOMIC_RELAXED, __HIP_MEMORY_SCOPE_WORKGROUP);
        }
        if (i == 7) { while (sy[1] < 8u * (l + 1)) __builtin_amdgcn_s_sleep(1); CFENCE(); }
        if (i >= 7 && i < 15) {
#pragma unroll
          for (int m = 0; m < 4; ++m) PF[(i - 7) * 4 + m] = WFRAG(i - 7, m);
        }
        SCHED();
#pragma unroll
        for (int m = 0; m < 4; ++m)
#pragma unroll
          for (int j = 0; j < 3; ++j) acc[m][j] = mfma(B[i % NB][j], PF[(i & 7) * 4 + m], acc[m][j]);
        SCHED();
        if ((i & 1) == 0) STAMP(3 + 3 * l, 3 + (i >> 1));
      }
    }
    STAMP(3 + 3 * l, 2);
    if (wv == 4) WOUT_PREFETCH();
    BAR();
#pragma unroll
    for (int m = 1; m < 4; ++m) {
      char* dst = smem + (((((m + w) & 3) * 2 + hi) * 3 + 3 - m) * 3) * 1024 + l16;
#pragma unroll
      for (int j = 0; j < 3; ++j) *(f32x4*)(dst + j * 1024) = acc[m][j];
    }
    BAR();
#pragma unroll
    for (int j = 0; j < 3; ++j) {
      f32x4 s = acc[0][j];
#pragma unroll
      for (int sw = 0; sw < 3; ++sw) s += *(const f32x4*)(smem + (((w * 2 + hi) * 3 + sw) * 3 + j) * 1024 + l16);
      u32x2 z1 = *(const u32x2*)(smem + L_Z1 + ((w * 6 + hi * 3 + j) * 64 + lane) * 8);
      const int nt = hi * 3 + j;
      float h0 = blo(z1[0]) * (s[0] + bt), h1 = bhi(z1[0]) * (s[1] + bt), h2 = blo(z1[1]) * (s[2] + bt), h3 = bhi(z1[1]) * (s[3] + bt);
      st64((u32x2){pack2(h0, h1), pack2(h2, h3)}, rs, e8, OFF_HF + l * SZZ + ((p * 24 + 3 * c + (nt >> 1)) * 4 + w) * 1024 + (nt & 1) * 512);
    }
    DRAIN();
    if (LAST_OF(4, 8 * (l + 1)) && lane == 0) __hip_atomic_store((GAS u32*)(ws + OFF_FL + FL2 + l * 4096 + p * 128) + c, 1u, RLX_AGENT);
    BAR();
    if (wv == 4) poll_flags(ws, OFF_FL + FL2 + l * 4096 + p * 128, 8, 1u, lane);
    else WOUT_PREFETCH();
    BAR();
    STAMP(3 + 3 * l, 15);
    }
    {
    IDS();
    STAMP(4 + 3 * l, 0);
    STAMP(4 + 3 * l, 1);
    {
      const int hoff = OFF_HF + l * SZZ + (p * 24 + wv) * 4096;
      u32x4 A[3][4];
#pragma unroll
      for (int i = 0; i < 3; ++i)
#pragma unroll
        for (int m = 0; m < 4; ++m) A[i][m] = ld128(rs, l16, hoff + i * 32768 + ((m + w) & 3) * 1024);
      SCHED();
      f32x4 a3[4][5];
#pragma unroll
      for (int m = 0; m < 4; ++m)
#pragma unroll
        for (int j = 0; j < 5; ++j) a3[m][j] = (f32x4){0, 0, 0, 0};
#pragma unroll
      for (int i = 0; i < 3; ++i)
#pragma unroll
        for (int m = 0; m < 4; ++m)
#pragma unroll
          for (int j = 0; j < 5; ++j) a3[m][j] = mfma(PF[16 + i * 5 + j], A[i][m], a3[m][j]);
      STAMP(4 + 3 * l, 2);
      SCHED();
      if (l == 0) {
        G1_PF_XY();
        if (wv == 4) G1_PF_W(0, G1SPLIT);
        else {
#pragma unroll
          for (int f = 0; f < 3 * G1SPLIT; ++f) PF[f] = (u32x4){0u, 0u, 0u, 0u};
        }
      }
#pragma unroll
      for (int m = 0; m < 4; ++m) {
        const int rt = (m + w) & 3;
        char* dlo = smem + (rt * 35 + ((wv - rt - 1) & 7)) * 1024 + l16;
        char* dhi = smem + (rt * 35 + ((wv - rt - 5) & 7)) * 1024 + l16;
        if (m != 0 || hi) {
#pragma unroll
          for (int j = 0; j < 3; ++j) *(f32x4*)(dlo + j * 7168) = a3[m][j];
        }
        if (m != 0 || !hi) {
#pragma unroll
          for (int j = 3; j < 5; ++j) *(f32x4*)(dhi + j * 7168) = a3[m][j];
        }
      }
      BAR();
      float s1 = 0.f, s2 = 0.f;
      auto fin3 = [&](auto JC) __attribute__((always_inline)) {
        constexpr int j = decltype(JC)::value;
        f32x4 s = a3[0][j];
#pragma unroll
        for (int sl = 0; sl < 7; ++sl) s += *(const f32x4*)(smem + (w * 35 + j * 7 + sl) * 1024 + l16);
        u32x2 xr = *(const u32x2*)(smem + L_X1 + ((w * 5 + j) * 64 + lane) * 8);
        u32x2 o = {pack2(blo(xr[0]) + s[0], bhi(xr[0]) + s[1]), pack2(blo(xr[1]) + s[2], bhi(xr[1]) + s[3])};
        if (l) {
          *(u32x2*)(out + (size_t)(p * 64 + w * 16 + r16) * D + c * 80 + j * 16 + 4 * g) = o;
        } else {
          const int nt = c * 5 + j;
          st64(o, rs, e8, OFF_X2F + ((p * 4 + w) * 20 + (nt >> 1)) * 1024 + (nt & 1) * 512);
          s1 = dot2(o[0], 0x3f803f80u, dot2(o[1], 0x3f803f80u, s1));
          s2 = dot2(o[0], o[0], dot2(o[1], o[1], s2));
        }
      };
      if (hi) { fin3(IC<3>{}); fin3(IC<4>{}); } else { fin3(IC<0>{}); fin3(IC<1>{}); fin3(IC<2>{}); }
      if (l) {
        STAMP(4 + 3 * l, 15);
        return;
      }
      s1 = add16(s1); s2 = add16(s2);
      s1 = add32(s1); s2 = add32(s2);
      if (lane < 16) st64((u32x2){__builtin_bit_cast(u32, s1), __builtin_bit_cast(u32, s2)}, rs, r16 * 128, OFF_XST + 262144 + (p * 64 + w * 16) * 128 + (c * 2 + hi) * 8);
      DRAIN();
      if (LAST_OF(5, 8) && lane == 0) __hip_atomic_store((GAS u32*)(ws + OFF_FL + FL3 + p * 128) + c, 1u, RLX_AGENT);
      BAR();
      if (wv == 4) poll_flags(ws, OFF_FL + FL3 + p * 128, 8, 1u, lane);
      else G1_PF_W(0, G1SPLIT);
      BAR();
    }
    STAMP(4 + 3 * l, 15);
    }
  };
  layer(IC<0>{});
  layer(IC<1>{});
}

extern "C" void kernel_launch(void* const* d_in, const int* in_sizes, int n_in, void* d_out, int out_size, void* d_ws, size_t ws_size,
                              hipStream_t stream) {
  const u16* q = (const u16*)d_in[0]; const u16* k = (const u16*)d_in[1]; const u16* v = (const u16*)d_in[2];
  const u16* sk = (const u16*)d_in[3]; const u16* hk = (const u16*)d_in[4]; const u16* Dv = (const u16*)d_in[5];
  const u16* win = (const u16*)d_in[7]; const u16* wsp = (const u16*)d_in[8]; const u16* bsp = (const u16*)d_in[9];
  const u16* wout = (const u16*)d_in[10];
  char* ws = (char*)d_ws;
  (void)hipFuncSetAttribute((const void*)k_main, hipFuncAttributeMaxDynamicSharedMemorySize, LDS_TOTAL);
  (void)hipFuncSetAttribute((const void*)k_front, hipFuncAttributeMaxDynamicSharedMemorySize, FRONT_LDS);
  k_front<<<256, 512, FRONT_LDS, stream>>>(q, k, v, sk, hk, Dv, win, wout, ws);
  k_main<<<256, 512, LDS_TOTAL, stream>>>(wsp, bsp, ws, (u16*)d_out);
}
```
